```python
import torch

def _softmax(x, axis=-1):
    return torch.softmax(x, dim=axis)
def _gelu(x):
    return torch.nn.functional.gelu(x, approximate="tanh")
def _silu(x):
    return torch.nn.functional.silu(x)
def _sigmoid(x):
    return torch.sigmoid(x)
def _layernorm(x, axis=-1, eps=1e-5):
    m = x.mean(dim=axis, keepdim=True)
    v = x.var(dim=axis, keepdim=True, unbiased=False)
    return (x - m) / torch.sqrt(v + eps)
def _scatter_add(tgt, idx, src, axis=0):
    return tgt.clone().index_add_(axis, idx, src)
def _segment_sum(src, seg, n):
    out = torch.zeros((n,) + src.shape[1:], dtype=src.dtype)
    return out.index_add_(0, seg, src)
def _pad(x, pw):
    flat = tuple(v for pair in reversed(pw) for v in pair)
    return torch.nn.functional.pad(x, flat)

kernel_name = 'concat_heads__gqa_block_cached__gqa_block_cached__near_ridge'

def setup_inputs():

    _w = (lambda a: (lambda t: t.to(torch.bfloat16) if t.is_floating_point() else t)(torch.as_tensor(a)))
    def _A_setup():
        B, Sq, D = 8, 64, 448
        Hq, Hkv, Sk, Dh = 16, 8, 64, 256
        assert Hq % Hkv == 0, (Hq, Hkv)
        return {
            "x": _w((lambda *s: torch.randn(*s, dtype=torch.bfloat16) * 0.8)(B, Sq, D)),
            "k_cache": _w((lambda *s: torch.randn(*s, dtype=torch.bfloat16) * 0.8)(B, Hkv, Sk, Dh)),
            "v_cache": _w((lambda *s: torch.randn(*s, dtype=torch.bfloat16) * 0.05)(B, Hkv, Sk, Dh)),
            "wq": _w((lambda *s: torch.randn(*s, dtype=torch.bfloat16) * 0.8)(D, Hq * Dh) * (D ** -0.5)),
            "wo": _w((lambda *s: torch.randn(*s, dtype=torch.bfloat16) * 0.05)(Hq * Dh, D) * ((Hq * Dh) ** -0.5)),
        }
    _A_in = _A_setup()

    def _B_setup():
        B, Sq, D = 8, 64, 448
        Hq, Hkv, Sk, Dh = 16, 8, 64, 256
        assert Hq % Hkv == 0, (Hq, Hkv)
        return {
            "x": _w((lambda *s: torch.randn(*s, dtype=torch.bfloat16) * 0.8)(B, Sq, D)),
            "k_cache": _w((lambda *s: torch.randn(*s, dtype=torch.bfloat16) * 0.8)(B, Hkv, Sk, Dh)),
            "v_cache": _w((lambda *s: torch.randn(*s, dtype=torch.bfloat16) * 0.05)(B, Hkv, Sk, Dh)),
            "wq": _w((lambda *s: torch.randn(*s, dtype=torch.bfloat16) * 0.8)(D, Hq * Dh) * (D ** -0.5)),
            "wo": _w((lambda *s: torch.randn(*s, dtype=torch.bfloat16) * 0.05)(Hq * Dh, D) * ((Hq * Dh) ** -0.5)),
        }
    _B_in = _B_setup()

    return {
        "A_x": _A_in["x"],
        "A_k_cache": _A_in["k_cache"],
        "A_v_cache": _A_in["v_cache"],
        "A_wq": _A_in["wq"],
        "A_wo": _A_in["wo"],
        "B_x": _B_in["x"],
        "B_k_cache": _B_in["k_cache"],
        "B_v_cache": _B_in["v_cache"],
        "B_wq": _B_in["wq"],
        "B_wo": _B_in["wo"],
    }

def reference(A_x, A_k_cache, A_v_cache, A_wq, A_wo, B_x, B_k_cache, B_v_cache, B_wq, B_wo):
    def _A_ref(x, k_cache, v_cache, wq, wo):
        B, Sq, D = x.shape
        Hq, Dh = 16, 256
        Hkv = k_cache.shape[1]
        rep = Hq // Hkv
        q = (lambda x, *d: x.permute(*d))((x @ wq).reshape(B, Sq, Hq, Dh), 0, 2, 1, 3)
        k = (lambda x, n, axis: torch.repeat_interleave(x, n, dim=axis))(k_cache, rep, axis=1)
        v = (lambda x, n, axis: torch.repeat_interleave(x, n, dim=axis))(v_cache, rep, axis=1)
        scores = torch.einsum("bhqd,bhkd->bhqk", q, k) * (Dh ** -0.5)
        p = _softmax(scores, axis=-1)
        o = torch.einsum("bhqk,bhkd->bhqd", p, v)
        return (lambda x, *d: x.permute(*d))(o, 0, 2, 1, 3).reshape(B, Sq, Hq * Dh) @ wo

    def _B_ref(x, k_cache, v_cache, wq, wo):
        B, Sq, D = x.shape
        Hq, Dh = 16, 256
        Hkv = k_cache.shape[1]
        rep = Hq // Hkv
        q = (lambda x, *d: x.permute(*d))((x @ wq).reshape(B, Sq, Hq, Dh), 0, 2, 1, 3)
        k = (lambda x, n, axis: torch.repeat_interleave(x, n, dim=axis))(k_cache, rep, axis=1)
        v = (lambda x, n, axis: torch.repeat_interleave(x, n, dim=axis))(v_cache, rep, axis=1)
        scores = torch.einsum("bhqd,bhkd->bhqk", q, k) * (Dh ** -0.5)
        p = _softmax(scores, axis=-1)
        o = torch.einsum("bhqk,bhkd->bhqd", p, v)
        return (lambda x, *d: x.permute(*d))(o, 0, 2, 1, 3).reshape(B, Sq, Hq * Dh) @ wo

    a = _A_ref(A_x, A_k_cache, A_v_cache, A_wq, A_wo)
    b = _B_ref(B_x, B_k_cache, B_v_cache, B_wq, B_wo)
    return torch.concatenate([a, b], axis=-1)
```

```cpp
#include <hip/hip_runtime.h>
#include <hip/hip_bf16.h>

typedef short s16x4 __attribute__((ext_vector_type(4)));
typedef short bf16x8 __attribute__((ext_vector_type(8)));
typedef float f32x4 __attribute__((ext_vector_type(4)));
typedef float f32x2 __attribute__((ext_vector_type(2)));
typedef float f32x16 __attribute__((ext_vector_type(16)));
typedef unsigned u32x4 __attribute__((ext_vector_type(4)));
typedef unsigned u32x2 __attribute__((ext_vector_type(2)));
typedef __bf16 bf16x2_t __attribute__((ext_vector_type(2)));
#define LAS __attribute__((address_space(3)))
#define GAS __attribute__((address_space(1)))
#define DI __device__ __forceinline__

#define ROT_B 2
#define ROT_H 2
#define ROT_Z 1

struct Params {
  const char* x0; const char* k0; const char* v0; const char* wq0; const char* wo0;
  const char* x1; const char* k1; const char* v1; const char* wq1; const char* wo1;
  char* out; char* ws;
};

constexpr unsigned WS_EPOCH = 0;
constexpr unsigned WS_PFLAG = 4096;
constexpr unsigned WS_FFLAG = 16384;
constexpr unsigned WS_P = 65536;
constexpr unsigned WS_F = WS_P + 32 * 65536;
constexpr unsigned WS_END = WS_F + 64 * 65536;

constexpr unsigned SW = 0;
constexpr unsigned SX = 131072;

#define ST(i) do {} while (0)
#define STDECL
#define STOUT() do {} while (0)

DI unsigned pk(float a, float b) { f32x2 v = {a, b}; return __builtin_bit_cast(unsigned, __builtin_convertvector(v, bf16x2_t)); }
DI bf16x8 pk8(f32x4 a, f32x4 b) { u32x4 r = {pk(a[0], a[1]), pk(a[2], a[3]), pk(b[0], b[1]), pk(b[2], b[3])}; return __builtin_bit_cast(bf16x8, r); }
DI bf16x8 ld128(unsigned a) { return *(LAS const bf16x8*)(size_t)a; }
DI bf16x8 ldtr(unsigned a0, unsigned a1) {
  s16x4 lo = __builtin_amdgcn_ds_read_tr16_b64_v4i16((LAS s16x4*)(size_t)a0);
  s16x4 hi = __builtin_amdgcn_ds_read_tr16_b64_v4i16((LAS s16x4*)(size_t)a1);
  return __builtin_shufflevector(lo, hi, 0, 1, 2, 3, 4, 5, 6, 7);
}
#define MFMA(a, b, c) __builtin_amdgcn_mfma_f32_16x16x32_bf16((a), (b), (c), 0, 0, 0)
#define MFMA32(a, b, c) __builtin_amdgcn_mfma_f32_32x32x16_bf16((a), (b), (c), 0, 0, 0)
#define ZERO4 (f32x4{0.f, 0.f, 0.f, 0.f})
#define ZERO16 (f32x16{0.f, 0.f, 0.f, 0.f, 0.f, 0.f, 0.f, 0.f, 0.f, 0.f, 0.f, 0.f, 0.f, 0.f, 0.f, 0.f})
DI bf16x8 pk16(const f32x16& x, int a) {
  u32x4 r = {pk(x[8 * a], x[8 * a + 1]), pk(x[8 * a + 2], x[8 * a + 3]), pk(x[8 * a + 4], x[8 * a + 5]), pk(x[8 * a + 6], x[8 * a + 7])};
  return __builtin_bit_cast(bf16x8, r);
}
#define BAR_C() do { __builtin_amdgcn_sched_barrier(0); asm volatile("" ::: "memory"); __builtin_amdgcn_s_waitcnt(0xC07F); \
                     __builtin_amdgcn_s_barrier(); asm volatile("" ::: "memory"); __builtin_amdgcn_sched_barrier(0); } while (0)
#define BAR_L(n) asm volatile("s_waitcnt vmcnt(" #n ")\n\ts_barrier" ::: "memory")
#define SG_MFMA(n) __builtin_amdgcn_sched_group_barrier(0x008, (n), 0)
#define SG_DSR(n) __builtin_amdgcn_sched_group_barrier(0x100, (n), 0)
#define SG_VALU(n) __builtin_amdgcn_sched_group_barrier(0x002, (n), 0)
#define NOLOAD 0
DI void glds(const char* sbase, unsigned voff, unsigned lds_addr) {
  if (NOLOAD) return;
  asm volatile("s_mov_b32 m0, %2\n\ts_nop 0\n\tglobal_load_lds_dwordx4 %0, %1" :: "v"(voff), "s"(sbase), "s"(lds_addr) : "memory");
}
DI void glds_dev(const char* sbase, unsigned voff, unsigned lds_addr) {
  asm volatile("s_mov_b32 m0, %2\n\ts_nop 0\n\tglobal_load_lds_dwordx4 %0, %1 sc1" :: "v"(voff), "s"(sbase), "s"(lds_addr) : "memory");
}
DI unsigned next_epoch(unsigned e) { return e * 0x9E3779B1u + 0x7F4A7C15u; }
DI float xmax(float v) {
  auto a = __builtin_amdgcn_permlane32_swap(__float_as_uint(v), __float_as_uint(v), false, false);
  v = fmaxf(__uint_as_float(a[0]), __uint_as_float(a[1]));
  auto b = __builtin_amdgcn_permlane16_swap(__float_as_uint(v), __float_as_uint(v), false, false);
  return fmaxf(__uint_as_float(b[0]), __uint_as_float(b[1]));
}
DI float xsum(float v) {
  auto a = __builtin_amdgcn_permlane32_swap(__float_as_uint(v), __float_as_uint(v), false, false);
  v = __uint_as_float(a[0]) + __uint_as_float(a[1]);
  auto b = __builtin_amdgcn_permlane16_swap(__float_as_uint(v), __float_as_uint(v), false, false);
  return __uint_as_float(b[0]) + __uint_as_float(b[1]);
}
constexpr unsigned SPIN_CAP = 1u << 18;

__global__ __launch_bounds__(512, 2) void gqa_fused(Params P) {
  __shared__ __attribute__((aligned(1024))) char lds[163840];
  const int tid = threadIdx.x, lane = tid & 63;
  const int w = __builtin_amdgcn_readfirstlane(tid >> 6);
  const int bid = blockIdx.x;
  const int xcd = bid & 7, sl = bid >> 3;
  const int br = xcd >> 2, gp = xcd & 3;
  const int bp = sl >> 3, m = sl & 7;
  const int hj = m & 3, h = gp * 4 + hj, b = bp * 2 + (m >> 2);
  const int team = xcd * 4 + bp, comp = br * 4 + bp, grp = comp * 8 + m;
  const unsigned lds0 = (unsigned)(size_t)(LAS char*)lds;
  const int g1 = ROT_Z ? (bp + m) & 1 : 0, k1 = ROT_Z ? (((bp + m) >> 1) & 1) * 2 + (m >> 2) : 0;
  STDECL;
  ST(0);

  if (w >= 4) {
    const int lw = w - 4;
    char* const ws = P.ws;
    GAS unsigned* const epoch_p = (GAS unsigned*)(ws + WS_EPOCH + comp * 256);
    GAS unsigned* const pflag_p = (GAS unsigned*)(ws + WS_PFLAG + team * 256);
    __builtin_amdgcn_s_setprio(3);
    const unsigned hi = lane >> 5, l31 = lane & 31;
    const unsigned v_x = (lane >> 3) * 896 + (((lane & 7) ^ (4 * (lw & 1) + (lane >> 4))) * 16);
    const unsigned v_wq = hi * 8192 + ((l31 ^ ((2 * (lw & 1) + hi) << 2)) * 16);
    const unsigned v_k = hi * 512 + ((l31 ^ (2 * lw + hi)) * 16);
    const unsigned v_wo = (lane >> 3) * 896 + (((lane & 7) ^ (((lane >> 4) & 1) << 2)) * 16);
    const unsigned v_v = (lane >> 3) * 512 + (((lane & 7) ^ (4 * (lw & 1) + (lane >> 4))) * 16);
    const unsigned v_k1 = v_k ^ 128;
    const char* const xb = (br ? P.x1 : P.x0) + b * 57344 + lw * (8 * 896);
    const char* const wqb = (br ? P.wq1 : P.wq0) + h * 512 + lw * (2 * 8192);
    const char* const kb = (br ? P.k1 : P.k0) + (b * 8 + (h >> 1)) * 32768 + lw * 1024;
    const char* const vb = (br ? P.v1 : P.v0) + (bp * 16 + gp * 2) * 32768 + lw * 4096;
    const char* const wob = (br ? P.wo1 : P.wo0) + gp * (4 * 229376) + m * 128 + lw * (8 * 896);
    int sa = ((ROT_B == 0 ? 0 : ROT_B == 1 ? b : (b >> 1) * 2) + ROT_H * hj);
    sa = sa >= 7 ? sa - 7 : sa; sa = sa >= 7 ? sa - 7 : sa;
    auto stageA = [&](int slot) {
      const char* wq = wqb + sa * (64 * 8192);
      const unsigned dw = lds0 + SW + slot * 32768 + lw * 1024;
      const char* xs = xb + sa * 128;
      const unsigned dx = lds0 + SX + slot * 8192 + lw * 1024;
      glds(xs, v_x, dx);
      glds(xs + 32 * 896, v_x, dx + 4096);
#pragma unroll
      for (int i = 0; i < 8; ++i) glds(wq + i * (8 * 8192), v_wq, dw + i * 4096);
      sa = sa == 6 ? 0 : sa + 1;
    };
    auto stageKV = [&](const char* src, int slot, int first) {
#pragma unroll
      for (int i = 0; i < 8; ++i) { const int ii = (i + 4 * first) & 7; glds(src + ii * 4096, (i & 1) ? v_k1 : v_k, lds0 + SW + slot * 32768 + lw * 1024 + ii * 4096); }
    };
    auto stageZ = [&](int z) {
      const int gl = g1 ^ (z >> 2), kq = (k1 + z) & 3;
      const char* sw = wob + gl * (2 * 229376) + kq * (64 * 896);
      const char* sv = vb + gl * 32768 + kq * 128;
      const unsigned d = lds0 + SW + ((z + 1) % 3) * 32768 + lw * 1024;
#pragma unroll
      for (int i = 0; i < 4; ++i) glds(sw + (i >> 1) * 229376 + (i & 1) * (32 * 896), v_wo, d + i * 4096);
#pragma unroll
      for (int i = 0; i < 4; ++i) glds(sv + (i >> 1) * (8 * 32768) + (i & 1) * 16384, v_v, d + 16384 + i * 4096);
    };
    stageA(0);
    unsigned ep;
    asm volatile("global_load_dword %0, %1, %2 sc1" : "=v"(ep) : "v"(0), "s"(epoch_p) : "memory");
    stageA(1);
    ST(10);
    BAR_L(11); stageA(2);
    ST(1);
#pragma unroll 1
    for (int t = 0, slot = 0; t < 4; ++t) {
      BAR_L(10); stageA(slot);
      slot = slot == 2 ? 0 : slot + 1;
    }
    asm volatile("" : "+v"(ep));
    BAR_L(10); stageKV(kb, 3, h & 1);
    if (m == 7) {
      BAR_L(8); BAR_L(63); BAR_L(0); BAR_L(0);
      STOUT();
      return;
    }
    BAR_L(8); stageZ(0);
    ST(2);
    BAR_L(63); stageZ(1);
    BAR_L(16); stageZ(2);
    ST(3);
    BAR_L(63);
    BAR_L(16);
    ST(4);
    BAR_L(8); stageZ(3);
    BAR_L(8); stageZ(4);
    ST(5);
    BAR_L(8); stageZ(5);
    unsigned fl;
    const unsigned voff_f = (lane & 31) * 4;
    asm volatile("global_load_dword %0, %1, %2 sc1" : "=v"(fl) : "v"(voff_f), "s"(pflag_p) : "memory");
    BAR_L(9); stageZ(6);
    ST(6);
    const unsigned want = next_epoch(__builtin_amdgcn_readfirstlane(ep));
    asm volatile("s_waitcnt vmcnt(8)" : "+v"(fl) :: "memory");
    for (unsigned spins = 0; !__all(fl == want) && spins < SPIN_CAP; ++spins)
      asm volatile("global_load_dword %0, %1, %2 sc1\n\ts_waitcnt vmcnt(0)" : "=v"(fl) : "v"(voff_f), "s"(pflag_p) : "memory");
    ST(7);
    {
      const char* src = ws + WS_P + team * 65536 + lw * 2048;
      const unsigned v_lin = lane * 16;
#pragma unroll 1
      for (int k = 1; k < 8; ++k) {
        const int mm = (m + k) & 7;
        const unsigned dst = lds0 + (mm < 4 ? SX + mm * 8192 : SW + 3 * 32768 + (mm - 4) * 8192) + lw * 2048;
        glds_dev(src + mm * 8192, v_lin, dst);
        glds_dev(src + mm * 8192 + 1024, v_lin, dst + 1024);
      }
    }
    BAR_L(22); stageZ(7);
    BAR_L(8);
    ST(8);
    BAR_L(0);
    ST(9);
    STOUT();
    return;
  }

  const unsigned r16 = lane & 15, g = lane >> 4, q4 = r16 >> 2, p4 = lane & 3;
  const unsigned r32 = lane & 31, hh = lane >> 5, blk = g & 1;
  char* const ws = P.ws;
  GAS unsigned* const epoch_p = (GAS unsigned*)(ws + WS_EPOCH + comp * 256);
  GAS unsigned* const pflag_p = (GAS unsigned*)(ws + WS_PFLAG + team * 256);
  const unsigned ep = __hip_atomic_load(epoch_p, __ATOMIC_RELAXED, __HIP_MEMORY_SCOPE_AGENT);
  const __amdgpu_buffer_rsrc_t rs = __builtin_amdgcn_make_buffer_rsrc(ws, 0, WS_END, 0x00020000);
  f32x16 acc[2][2];
  bf16x8 fa0[2][2], fb0[2][2], fa1[2][2], fb1[2][2];
  auto mm = [&](const bf16x8 (&fa)[2][2], const bf16x8 (&fb)[2][2], bool first) {
#pragma unroll
    for (int s = 0; s < 2; ++s)
#pragma unroll
      for (int i = 0; i < 2; ++i)
#pragma unroll
        for (int j = 0; j < 2; ++j) acc[i][j] = MFMA32(fa[s][i], fb[s][j], (first && s == 0) ? ZERO16 : acc[i][j]);
  };
#define SG_HALF() do { _Pragma("unroll") for (int i_ = 0; i_ < 4; ++i_) { SG_MFMA(1); SG_DSR(3); } SG_MFMA(4); } while (0)
#define PIN_ACC() asm volatile("" : "+v"(acc[0][0]), "+v"(acc[0][1]), "+v"(acc[1][0]), "+v"(acc[1][1]))
  {
    const unsigned xa = lds0 + SX + r32 * 128 + ((hh ^ ((r32 >> 1) & 7)) * 16);
    const unsigned wa = lds0 + SW + (8 * hh + q4) * 512 + (((8 * w + 2 * blk + (p4 >> 1)) ^ (q4 << 2)) * 16) + (p4 & 1) * 8;
    auto rd = [&](bf16x8 (&fa)[2][2], bf16x8 (&fb)[2][2], int slot, int half) {
#pragma unroll
      for (int s = 0; s < 2; ++s)
#pragma unroll
        for (int i = 0; i < 2; ++i) {
          const int ks = 2 * half + s;
          const unsigned wb = (wa ^ (i * 64)) + (slot == 2 ? 65536 : 0), wo_ = (slot == 1 ? 32768 : 0) + ks * 8192;
          fa[s][i] = ldtr(wb + wo_, wb + wo_ + 2048);
          fb[s][i] = ld128((xa ^ (ks * 32)) + slot * 8192 + i * 4096);
        }
    };
    BAR_C();
    ST(1);
    rd(fa0, fb0, 0, 0);
#pragma unroll
    for (int t = 0; t < 7; ++t) {
      rd(fa1, fb1, t % 3, 1);
      mm(fa0, fb0, t == 0);
      SG_HALF();
      PIN_ACC();
      BAR_C();
      if (t < 6) rd(fa0, fb0, (t + 1) % 3, 0);
      mm(fa1, fb1, false);
      if (t < 6) { SG_HALF(); PIN_ACC(); __builtin_amdgcn_sched_barrier(0); }
    }
  }
  ST(2);
  {
    const unsigned qa = lds0 + SX + (r32 >> 4) * 8192 + w * 2048 + r16 * 16 + hh * 8;
#pragma unroll
    for (int i = 0; i < 2; ++i)
#pragma unroll
      for (int j = 0; j < 2; ++j)
#pragma unroll
        for (int a = 0; a < 4; ++a)
          *(LAS u32x2*)(size_t)(qa + j * 16384 + i * 1024 + a * 256) = u32x2{pk(acc[i][j][4 * a], acc[i][j][4 * a + 1]), pk(acc[i][j][4 * a + 2], acc[i][j][4 * a + 3])};
  }
  BAR_C();
  ST(3);
  const unsigned want = next_epoch(ep);
  {
    f32x4 sc[4];
#pragma unroll
    for (int i = 0; i < 4; ++i) sc[i] = ZERO4;
    const unsigned own = lds0 + SX + w * 8192 + lane * 16;
    const unsigned ka = lds0 + SW + 3 * 32768 + r16 * 512 + ((g ^ r16) * 16);
#pragma unroll
    for (int s = 0; s < 8; ++s) {
      const bf16x8 qf = ld128(own + s * 1024);
#pragma unroll
      for (int kt = 0; kt < 4; ++kt) sc[kt] = MFMA(ld128((ka ^ ((s & 3) * 64)) + kt * 8192 + (s >> 2) * 256), qf, sc[kt]);
    }
    SG_VALU(4); SG_DSR(10);
#pragma unroll
    for (int i = 0; i < 30; ++i) { SG_MFMA(1); SG_DSR(1); }
    SG_MFMA(2);
    BAR_C();
    ST(4);
    float mx = fmaxf(fmaxf(fmaxf(sc[0][0], sc[0][1]), fmaxf(sc[0][2], sc[0][3])), fmaxf(fmaxf(sc[1][0], sc[1][1]), fmaxf(sc[1][2], sc[1][3])));
    mx = fmaxf(mx, fmaxf(fmaxf(fmaxf(sc[2][0], sc[2][1]), fmaxf(sc[2][2], sc[2][3])), fmaxf(fmaxf(sc[3][0], sc[3][1]), fmaxf(sc[3][2], sc[3][3]))));
    mx = xmax(mx);
    constexpr float C = 0.0625f * 1.4426950408889634f;
    const float mc = mx * C;
    float l = 0.f;
#pragma unroll
    for (int kt = 0; kt < 4; ++kt)
#pragma unroll
      for (int r = 0; r < 4; ++r) { sc[kt][r] = __builtin_amdgcn_exp2f(fmaf(sc[kt][r], C, -mc)); l += sc[kt][r]; }
    l = xsum(l);
    const float inv = __builtin_amdgcn_rcpf(l);
    const unsigned po = (w >> 1) * 4096 + hh * 1024 + ((w & 1) * 16 + r16 + 32 * blk) * 16;
    const unsigned go = WS_P + team * 65536 + m * 8192 + po;
    const unsigned lo = lds0 + (m < 4 ? SX + m * 8192 : SW + 3 * 32768 + (m - 4) * 8192) + po;
#pragma unroll
    for (int e = 0; e < 2; ++e) {
      const f32x4 a = sc[2 * e] * inv, c = sc[2 * e + 1] * inv;
      const auto s0 = __builtin_amdgcn_permlane32_swap(pk(a[0], a[1]), pk(c[0], c[1]), false, false);
      const auto s1 = __builtin_amdgcn_permlane32_swap(pk(a[2], a[3]), pk(c[2], c[3]), false, false);
      const u32x4 v = {s0[0], s1[0], s0[1], s1[1]};
      __builtin_amdgcn_raw_buffer_store_b128(v, rs, go + e * 2048, 0, 16);
      *(LAS u32x4*)(size_t)(lo + e * 2048) = v;
    }
  }
  ST(5);
  if (m == 7) {
    asm volatile("s_waitcnt vmcnt(0)" ::: "memory");
    if (lane == 0) __hip_atomic_store(pflag_p + m * 4 + w, want, __ATOMIC_RELAXED, __HIP_MEMORY_SCOPE_AGENT);
    STOUT();
    return;
  }
  const int ct = w & 1, bi = w >> 1;
  f32x16 o[2];
  {
    const unsigned wa = lds0 + SW + (8 * hh + q4) * 128 + (((4 * ct + 2 * blk + (p4 >> 1)) ^ ((q4 >> 1) << 2)) * 16) + (p4 & 1) * 8;
    const unsigned va = lds0 + SW + 16384 + bi * 8192 + r32 * 128 + ((hh ^ ((r32 >> 1) & 7)) * 16);
    auto rd = [&](bf16x8 (&fa)[2][2], bf16x8 (&fb)[2][2], int slot, int half) {
#pragma unroll
      for (int s = 0; s < 2; ++s)
#pragma unroll
        for (int i = 0; i < 2; ++i) {
          const int ks = 2 * half + s;
          const unsigned hi = slot == 2 ? 65536 : 0, lo = slot == 1 ? 32768 : 0;
          fa[s][i] = ld128(((va ^ (ks * 32)) + hi) + lo + i * 4096);
          fb[s][i] = ldtr((wa + hi) + lo + i * 8192 + ks * 2048, (wa + hi) + lo + i * 8192 + ks * 2048 + 512);
        }
    };
    auto packC = [&](bf16x8 (&cf)[2][4]) {
#pragma unroll
      for (int e = 0; e < 2; ++e)
#pragma unroll
        for (int s = 0; s < 4; ++s) cf[e][s] = pk16(acc[s >> 1][e], s & 1);
    };
    auto compPC = [&](const bf16x8 (&cf)[2][4], int gl, bool first) {
      const unsigned pa = lds0 + (bi ? SW + 3 * 32768 : SX) + gl * 16384 + lane * 16;
#pragma unroll
      for (int e = 0; e < 2; ++e)
#pragma unroll
        for (int s = 0; s < 4; ++s)
#pragma unroll
          for (int qt = 0; qt < 2; ++qt) o[qt] = MFMA32(cf[e][s], ld128(pa + e * 8192 + qt * 4096 + s * 1024), (first && e == 0 && s == 0) ? ZERO16 : o[qt]);
    };
    bf16x8 cf0[2][4], cf1[2][4];
    BAR_C();
    ST(6);
    rd(fa0, fb0, 1, 0);
#pragma unroll
    for (int z = 0; z < 8; ++z) {
      rd(fa1, fb1, (z + 1) % 3, 1);
      mm(fa0, fb0, (z & 3) == 0);
      SG_HALF();
      PIN_ACC();
      if (z == 0) {
        asm volatile("s_waitcnt vmcnt(0)" ::: "memory");
        if (lane == 0) __hip_atomic_store(pflag_p + m * 4 + w, want, __ATOMIC_RELAXED, __HIP_MEMORY_SCOPE_AGENT);
      }
      if (z < 7) {
        BAR_C();
        if (z == 4) ST(8);
        if (z == 5) ST(9);
        if (z == 6) ST(10);
        rd(fa0, fb0, (z + 2) % 3, 0);
      }
      mm(fa1, fb1, false);
      if (z < 7) SG_HALF();
      if (z == 3) { packC(cf0); asm volatile("" : "+v"(cf0[0][0]), "+v"(cf0[0][1]), "+v"(cf0[0][2]), "+v"(cf0[0][3]), "+v"(cf0[1][0]), "+v"(cf0[1][1]), "+v"(cf0[1][2]), "+v"(cf0[1][3])); }
      else if (z < 7) PIN_ACC();
      __builtin_amdgcn_sched_barrier(0);
      if (z == 5) {
        compPC(cf0, g1, true);
        SG_DSR(4);
#pragma unroll
        for (int i = 0; i < 12; ++i) { SG_MFMA(1); SG_DSR(1); }
        SG_MFMA(4);
        asm volatile("" : "+v"(o[0]), "+v"(o[1]));
        __builtin_amdgcn_sched_barrier(0);
      }
    }
    packC(cf1);
    compPC(cf1, g1 ^ 1, false);
  }
  ST(11);
  const unsigned fo = WS_F + grp * 65536 + gp * 4096 + w * 1024 + lane * 16;
  GAS unsigned* const fflag_p = (GAS unsigned*)(ws + WS_FFLAG + grp * 256);
#pragma unroll
  for (int k = 0; k < 4; ++k)
    if (gp != k) __builtin_amdgcn_raw_buffer_store_b128(__builtin_bit_cast(u32x4, pk16(o[k >> 1], k & 1)), rs, fo + k * 16384, 0, 16);
  asm volatile("s_waitcnt vmcnt(0)" ::: "memory");
  if (lane == 0) __hip_atomic_store(fflag_p + gp * 4 + w, want, __ATOMIC_RELAXED, __HIP_MEMORY_SCOPE_AGENT);
  ST(12);
  {
    float a[8];
#pragma unroll
    for (int i = 0; i < 8; ++i) a[i] = gp == 0 ? o[0][i] : gp == 1 ? o[0][8 + i] : gp == 2 ? o[1][i] : o[1][8 + i];
    GAS unsigned* const fp = fflag_p + (lane & 3) * 4 + w;
    for (unsigned spins = 0; spins < SPIN_CAP; ++spins) {
      const unsigned fl = __hip_atomic_load(fp, __ATOMIC_RELAXED, __HIP_MEMORY_SCOPE_AGENT);
      if (__all(fl == want || (lane & 3) == gp)) break;
    }
    asm volatile("" ::: "memory");
    ST(13);
    const unsigned fi = WS_F + grp * 65536 + gp * 16384 + w * 1024 + lane * 16;
    u32x4 d[3];
#pragma unroll
    for (int k = 0; k < 3; ++k) d[k] = __builtin_amdgcn_raw_buffer_load_b128(rs, fi + ((gp + 1 + k) & 3) * 4096, 0, 16);
#pragma unroll
    for (int k = 0; k < 3; ++k)
#pragma unroll
      for (int i = 0; i < 4; ++i) { a[2 * i] += __uint_as_float(d[k][i] << 16); a[2 * i + 1] += __uint_as_float(d[k][i] & 0xffff0000u); }
    const auto s0 = __builtin_amdgcn_permlane32_swap(pk(a[0], a[1]), pk(a[4], a[5]), false, false);
    const auto s1 = __builtin_amdgcn_permlane32_swap(pk(a[2], a[3]), pk(a[6], a[7]), false, false);
    char* op = P.out + (((bp * 2 + bi) * 64 + (gp >> 1) * 32 + r32) * 896 + br * 448 + m * 64 + ct * 32 + (gp & 1) * 16 + hh * 8) * 2;
    *(u32x4*)op = u32x4{s0[0], s1[0], s0[1], s1[1]};
    if (gp == 2 && m == 0 && w == 0 && lane == 0) __hip_atomic_store(epoch_p, want, __ATOMIC_RELAXED, __HIP_MEMORY_SCOPE_AGENT);
  }
  ST(14);
  STOUT();
}

extern "C" void kernel_launch(void* const* d_in, const int* in_sizes, int n_in, void* d_out, int out_size,
                              void* d_ws, size_t ws_size, hipStream_t stream) {
  Params p;
  p.x0 = (const char*)d_in[0]; p.k0 = (const char*)d_in[1]; p.v0 = (const char*)d_in[2]; p.wq0 = (const char*)d_in[3]; p.wo0 = (const char*)d_in[4];
  p.x1 = (const char*)d_in[5]; p.k1 = (const char*)d_in[6]; p.v1 = (const char*)d_in[7]; p.wq1 = (const char*)d_in[8]; p.wo1 = (const char*)d_in[9];
  p.out = (char*)d_out; p.ws = (char*)d_ws;
  gqa_fused<<<256, 512, 0, stream>>>(p);
}
```

```cpp
#include <hip/hip_runtime.h>
#include <hip/hip_bf16.h>

typedef short s16x4 __attribute__((ext_vector_type(4)));
typedef short bf16x8 __attribute__((ext_vector_type(8)));
typedef float f32x4 __attribute__((ext_vector_type(4)));
typedef float f32x2 __attribute__((ext_vector_type(2)));
typedef float f32x16 __attribute__((ext_vector_type(16)));
typedef unsigned u32x4 __attribute__((ext_vector_type(4)));
typedef unsigned u32x2 __attribute__((ext_vector_type(2)));
typedef __bf16 bf16x2_t __attribute__((ext_vector_type(2)));
#define LAS __attribute__((address_space(3)))
#define GAS __attribute__((address_space(1)))
#define CAS __attribute__((address_space(4)))
#define DI __device__ __forceinline__

#define ROT_B 2
#define ROT_H 2
#define POL ""
#define PFLAG_AT 1
#define ROT_Z 1

struct Params {
  const char* x0; const char* k0; const char* v0; const char* wq0; const char* wo0;
  const char* x1; const char* k1; const char* v1; const char* wq1; const char* wo1;
  char* out; char* ws;
};

constexpr unsigned WS_EPOCH = 0;
constexpr unsigned WS_PFLAG = 4096;
constexpr unsigned WS_FFLAG = 16384;
constexpr unsigned WS_P = 65536;
constexpr unsigned WS_F = WS_P + 32 * 65536;
constexpr unsigned WS_END = WS_F + 64 * 65536;

constexpr unsigned SW = 0;
constexpr unsigned SX = 131072;

#define ST(i) do {} while (0)
#define STDECL
#define STOUT() do {} while (0)

DI unsigned pk(float a, float b) { f32x2 v = {a, b}; return __builtin_bit_cast(unsigned, __builtin_convertvector(v, bf16x2_t)); }
DI bf16x8 pk8(f32x4 a, f32x4 b) { u32x4 r = {pk(a[0], a[1]), pk(a[2], a[3]), pk(b[0], b[1]), pk(b[2], b[3])}; return __builtin_bit_cast(bf16x8, r); }
DI bf16x8 ld128(unsigned a) { return *(LAS const bf16x8*)(size_t)a; }
DI bf16x8 ldtr(unsigned a0, unsigned a1) {
  s16x4 lo = __builtin_amdgcn_ds_read_tr16_b64_v4i16((LAS s16x4*)(size_t)a0);
  s16x4 hi = __builtin_amdgcn_ds_read_tr16_b64_v4i16((LAS s16x4*)(size_t)a1);
  return __builtin_shufflevector(lo, hi, 0, 1, 2, 3, 4, 5, 6, 7);
}
#define MFMA(a, b, c) __builtin_amdgcn_mfma_f32_16x16x32_bf16((a), (b), (c), 0, 0, 0)
#define MFMA32(a, b, c) __builtin_amdgcn_mfma_f32_32x32x16_bf16((a), (b), (c), 0, 0, 0)
#define ZERO4 (f32x4{0.f, 0.f, 0.f, 0.f})
#define ZERO16 (f32x16{0.f, 0.f, 0.f, 0.f, 0.f, 0.f, 0.f, 0.f, 0.f, 0.f, 0.f, 0.f, 0.f, 0.f, 0.f, 0.f})
DI bf16x8 pk16(const f32x16& x, int a) {
  u32x4 r = {pk(x[8 * a], x[8 * a + 1]), pk(x[8 * a + 2], x[8 * a + 3]), pk(x[8 * a + 4], x[8 * a + 5]), pk(x[8 * a + 6], x[8 * a + 7])};
  return __builtin_bit_cast(bf16x8, r);
}
#define BAR_C() do { __builtin_amdgcn_sched_barrier(0); asm volatile("" ::: "memory"); __builtin_amdgcn_s_waitcnt(0xC07F); \
                     __builtin_amdgcn_s_barrier(); asm volatile("" ::: "memory"); __builtin_amdgcn_sched_barrier(0); } while (0)
#define BAR_L(n) asm volatile("s_waitcnt vmcnt(" #n ")\n\ts_barrier" ::: "memory")
#define SG_MFMA(n) __builtin_amdgcn_sched_group_barrier(0x008, (n), 0)
#define SG_DSR(n) __builtin_amdgcn_sched_group_barrier(0x100, (n), 0)
#define SG_VALU(n) __builtin_amdgcn_sched_group_barrier(0x002, (n), 0)
#define NOLOAD 0
DI void glds(const char* sbase, unsigned voff, unsigned lds_addr) {
  if (NOLOAD) return;
  asm volatile("s_mov_b32 m0, %2\n\ts_nop 0\n\tglobal_load_lds_dwordx4 %0, %1" POL :: "v"(voff), "s"(sbase), "s"(lds_addr) : "memory");
}
DI void glds_dev(const char* sbase, unsigned voff, unsigned lds_addr) {
  asm volatile("s_mov_b32 m0, %2\n\ts_nop 0\n\tglobal_load_lds_dwordx4 %0, %1 sc1" :: "v"(voff), "s"(sbase), "s"(lds_addr) : "memory");
}
DI unsigned next_epoch(unsigned e) { return e * 0x9E3779B1u + 0x7F4A7C15u; }
DI float xmax(float v) {
  auto a = __builtin_amdgcn_permlane32_swap(__float_as_uint(v), __float_as_uint(v), false, false);
  v = fmaxf(__uint_as_float(a[0]), __uint_as_float(a[1]));
  auto b = __builtin_amdgcn_permlane16_swap(__float_as_uint(v), __float_as_uint(v), false, false);
  return fmaxf(__uint_as_float(b[0]), __uint_as_float(b[1]));
}
DI float xsum(float v) {
  auto a = __builtin_amdgcn_permlane32_swap(__float_as_uint(v), __float_as_uint(v), false, false);
  v = __uint_as_float(a[0]) + __uint_as_float(a[1]);
  auto b = __builtin_amdgcn_permlane16_swap(__float_as_uint(v), __float_as_uint(v), false, false);
  return __uint_as_float(b[0]) + __uint_as_float(b[1]);
}
constexpr int zslot(int z) { return (0x10213021 >> (4 * z)) & 3; }
constexpr unsigned SPIN_CAP = 1u << 18;

__global__ __launch_bounds__(512, 2) void gqa_fused(Params P) {
  __shared__ __attribute__((aligned(1024))) char lds[163840];
  const int tid = threadIdx.x, lane = tid & 63;
  const int w = __builtin_amdgcn_readfirstlane(tid >> 6);
  const int bid = blockIdx.x;
  const int xcd = bid & 7, sl = bid >> 3;
  const int br = xcd >> 2, gp = xcd & 3;
  const int bp = sl >> 3, m = sl & 7;
  const int hj = m & 3, h = gp * 4 + hj, b = bp * 2 + (m >> 2);
  const int team = xcd * 4 + bp, comp = br * 4 + bp, grp = comp * 8 + m;
  const unsigned lds0 = (unsigned)(size_t)(LAS char*)lds;
  const int g1 = ROT_Z ? (bp + m) & 1 : 0, k1 = ROT_Z ? (((bp + m) >> 1) & 1) * 2 + (m >> 2) : 0;
  STDECL;
  ST(0);

  if (w >= 4) {
    const int lw = w - 4;
    char* const ws = P.ws;
    GAS unsigned* const epoch_p = (GAS unsigned*)(ws + WS_EPOCH + comp * 256);
    GAS unsigned* const pflag_p = (GAS unsigned*)(ws + WS_PFLAG + team * 256);
    __builtin_amdgcn_s_setprio(3);
    unsigned ep = *(const CAS unsigned*)(size_t)epoch_p;
    const unsigned hi = lane >> 5, l31 = lane & 31;
    const unsigned v_x = (lane >> 3) * 896 + (((lane & 7) ^ (4 * (lw & 1) + (lane >> 4))) * 16);
    const unsigned v_wq = hi * 8192 + ((l31 ^ ((2 * (lw & 1) + hi) << 2)) * 16);
    const unsigned v_k = hi * 512 + ((l31 ^ (2 * lw + hi)) * 16);
    const unsigned v_wo = (lane >> 3) * 896 + (((lane & 7) ^ (((lane >> 4) & 1) << 2)) * 16);
    const unsigned v_v = (lane >> 3) * 512 + (((lane & 7) ^ (4 * (lw & 1) + (lane >> 4))) * 16);
    const unsigned v_k1 = v_k ^ 128;
    const char* const xb = (br ? P.x1 : P.x0) + b * 57344 + lw * (8 * 896);
    const char* const wqb = (br ? P.wq1 : P.wq0) + h * 512 + lw * (2 * 8192);
    const char* const kb = (br ? P.k1 : P.k0) + (b * 8 + (h >> 1)) * 32768 + lw * 1024;
    const char* const vb = (br ? P.v1 : P.v0) + (bp * 16 + gp * 2) * 32768 + lw * 4096;
    const char* const wob = (br ? P.wo1 : P.wo0) + gp * (4 * 229376) + m * 128 + lw * (8 * 896);
    int sa = ((ROT_B == 0 ? 0 : ROT_B == 1 ? b : (b >> 1) * 2) + ROT_H * hj);
    sa = sa >= 7 ? sa - 7 : sa; sa = sa >= 7 ? sa - 7 : sa;
    auto stageA = [&](int slot) {
      const char* wq = wqb + sa * (64 * 8192);
      const unsigned dw = lds0 + SW + slot * 32768 + lw * 1024;
      const char* xs = xb + sa * 128;
      const unsigned dx = lds0 + SX + slot * 8192 + lw * 1024;
      glds(xs, v_x, dx);
      glds(xs + 32 * 896, v_x, dx + 4096);
#pragma unroll
      for (int i = 0; i < 8; ++i) glds(wq + i * (8 * 8192), v_wq, dw + i * 4096);
      sa = sa == 6 ? 0 : sa + 1;
    };
    auto stageKV = [&](const char* src, int slot, int first) {
#pragma unroll
      for (int i = 0; i < 8; ++i) { const int ii = (i + 4 * first) & 7; glds(src + ii * 4096, (i & 1) ? v_k1 : v_k, lds0 + SW + slot * 32768 + lw * 1024 + ii * 4096); }
    };
    auto stageZ = [&](int z, int part) {
      const int gl = g1 ^ (z >> 2), kq = (k1 + z) & 3;
      const char* sw = wob + gl * (2 * 229376) + kq * (64 * 896);
      const char* sv = vb + gl * 32768 + kq * 128;
      const unsigned d = lds0 + SW + zslot(z) * 32768 + lw * 1024;
      if (part & 1) {
#pragma unroll
        for (int i = 0; i < 4; ++i) glds(sw + (i >> 1) * 229376 + (i & 1) * (32 * 896), v_wo, d + i * 4096);
      }
      if (part & 2) {
#pragma unroll
        for (int i = 0; i < 4; ++i) glds(sv + (i >> 1) * (8 * 32768) + (i & 1) * 16384, v_v, d + 16384 + i * 4096);
      }
    };
    stageA(0);
    stageA(1);
    ST(10);
    BAR_L(10); stageA(2);
    asm volatile("" : "+s"(ep));
    ST(1);
#pragma unroll 1
    for (int t = 0; t < 4; ++t) {
      BAR_L(10); stageA(t == 3 ? 0 : t);
    }
    BAR_L(10); stageKV(kb, 3, h & 1);
    if (m == 7) {
      BAR_L(8); BAR_L(63); BAR_L(0);
      STOUT();
      return;
    }
    BAR_L(8); stageZ(0, 3);
    ST(2);
    BAR_L(63);
    BAR_L(8); stageZ(1, 3); stageZ(2, 1);
    ST(3);
    BAR_L(12); stageZ(2, 2); stageZ(3, 1);
    ST(4);
    BAR_L(12); stageZ(3, 2); stageZ(4, 1);
    BAR_L(12); stageZ(4, 2); stageZ(5, 1);
    ST(5);
    unsigned fl;
    const unsigned voff_f = (lane & 31) * 4;
    BAR_L(12);
    asm volatile("global_load_dword %0, %1, %2 sc1" : "=v"(fl) : "v"(voff_f), "s"(pflag_p) : "memory");
    stageZ(5, 2); stageZ(6, 1);
    BAR_L(13); stageZ(6, 2);
    ST(6);
    const unsigned want = next_epoch(ep);
    asm volatile("s_waitcnt vmcnt(12)" : "+v"(fl) :: "memory");
    for (unsigned spins = 0; !__all(fl == want) && spins < SPIN_CAP; ++spins)
      asm volatile("global_load_dword %0, %1, %2 sc1\n\ts_waitcnt vmcnt(0)" : "=v"(fl) : "v"(voff_f), "s"(pflag_p) : "memory");
    ST(7);
    {
      const char* src = ws + WS_P + team * 65536 + lw * 2048;
      const unsigned v_lin = lane * 16;
#pragma unroll 1
      for (int k = 1; k < 8; ++k) {
        const int mm = (m + k) & 7;
        const unsigned dst = lds0 + (((mm ^ m) & 4) ? SW + 3 * 32768 : SX) + (mm & 3) * 8192 + lw * 2048;
        glds_dev(src + mm * 8192, v_lin, dst);
        glds_dev(src + mm * 8192 + 1024, v_lin, dst + 1024);
      }
    }
    BAR_L(22); stageZ(7, 3);
    BAR_L(8);
    ST(8);
    BAR_L(0);
    ST(9);
    STOUT();
    return;
  }

  const unsigned r16 = lane & 15, g = lane >> 4, q4 = r16 >> 2, p4 = lane & 3;
  const unsigned r32 = lane & 31, hh = lane >> 5, blk = g & 1;
  char* const ws = P.ws;
  GAS unsigned* const epoch_p = (GAS unsigned*)(ws + WS_EPOCH + comp * 256);
  GAS unsigned* const pflag_p = (GAS unsigned*)(ws + WS_PFLAG + team * 256);
  const unsigned ep = *(const CAS unsigned*)(size_t)epoch_p;
  const __amdgpu_buffer_rsrc_t rs = __builtin_amdgcn_make_buffer_rsrc(ws, 0, WS_END, 0x00020000);
  f32x16 acc[2][2];
  bf16x8 fa0[2][2], fb0[2][2], fa1[2][2], fb1[2][2];
  auto mm = [&](const bf16x8 (&fa)[2][2], const bf16x8 (&fb)[2][2], bool first) {
#pragma unroll
    for (int s = 0; s < 2; ++s)
#pragma unroll
      for (int i = 0; i < 2; ++i)
#pragma unroll
        for (int j = 0; j < 2; ++j) acc[i][j] = MFMA32(fa[s][i], fb[s][j], (first && s == 0) ? ZERO16 : acc[i][j]);
  };
#define SG_HALF() do { _Pragma("unroll") for (int i_ = 0; i_ < 4; ++i_) { SG_MFMA(1); SG_DSR(3); } SG_MFMA(4); } while (0)
#define PIN_ACC() asm volatile("" : "+v"(acc[0][0]), "+v"(acc[0][1]), "+v"(acc[1][0]), "+v"(acc[1][1]))
  {
    const unsigned xa = lds0 + SX + r32 * 128 + ((hh ^ ((r32 >> 1) & 7)) * 16);
    const unsigned wa = lds0 + SW + (8 * hh + q4) * 512 + (((8 * w + 2 * blk + (p4 >> 1)) ^ (q4 << 2)) * 16) + (p4 & 1) * 8;
    auto rd = [&](bf16x8 (&fa)[2][2], bf16x8 (&fb)[2][2], int slot, int half) {
#pragma unroll
      for (int s = 0; s < 2; ++s)
#pragma unroll
        for (int i = 0; i < 2; ++i) {
          const int ks = 2 * half + s;
          const unsigned wb = (wa ^ (i * 64)) + (slot >> 1) * 65536, wo_ = (slot & 1) * 32768 + ks * 8192;
          fa[s][i] = ldtr(wb + wo_, wb + wo_ + 2048);
          fb[s][i] = ld128((xa ^ (ks * 32)) + slot * 8192 + i * 4096);
        }
    };
    BAR_C();
    ST(1);
    rd(fa0, fb0, 0, 0);
#pragma unroll
    for (int t = 0; t < 7; ++t) {
      rd(fa1, fb1, t % 3, 1);
      mm(fa0, fb0, t == 0);
      SG_HALF();
      PIN_ACC();
      BAR_C();
      if (t < 6) rd(fa0, fb0, (t + 1) % 3, 0);
      mm(fa1, fb1, false);
      if (t < 6) { SG_HALF(); PIN_ACC(); __builtin_amdgcn_sched_barrier(0); }
    }
  }
  ST(2);
  {
    const unsigned qa = lds0 + SX + (r32 >> 4) * 8192 + w * 2048 + r16 * 16 + hh * 8;
#pragma unroll
    for (int i = 0; i < 2; ++i)
#pragma unroll
      for (int j = 0; j < 2; ++j)
#pragma unroll
        for (int a = 0; a < 4; ++a)
          *(LAS u32x2*)(size_t)(qa + j * 16384 + i * 1024 + a * 256) = u32x2{pk(acc[i][j][4 * a], acc[i][j][4 * a + 1]), pk(acc[i][j][4 * a + 2], acc[i][j][4 * a + 3])};
  }
  BAR_C();
  ST(3);
  const unsigned want = next_epoch(ep);
  u32x4 pv[2];
  unsigned plo;
  {
    f32x4 sc[4];
#pragma unroll
    for (int i = 0; i < 4; ++i) sc[i] = ZERO4;
    const unsigned own = lds0 + SX + w * 8192 + lane * 16;
    const unsigned ka = lds0 + SW + 3 * 32768 + r16 * 512 + ((g ^ r16) * 16);
#pragma unroll
    for (int s = 0; s < 8; ++s) {
      const bf16x8 qf = ld128(own + s * 1024);
#pragma unroll
      for (int kt = 0; kt < 4; ++kt) sc[kt] = MFMA(ld128((ka ^ ((s & 3) * 64)) + kt * 8192 + (s >> 2) * 256), qf, sc[kt]);
    }
    SG_VALU(4); SG_DSR(10);
#pragma unroll
    for (int i = 0; i < 30; ++i) { SG_MFMA(1); SG_DSR(1); }
    SG_MFMA(2);
    ST(4);
    float mx = fmaxf(fmaxf(fmaxf(sc[0][0], sc[0][1]), fmaxf(sc[0][2], sc[0][3])), fmaxf(fmaxf(sc[1][0], sc[1][1]), fmaxf(sc[1][2], sc[1][3])));
    mx = fmaxf(mx, fmaxf(fmaxf(fmaxf(sc[2][0], sc[2][1]), fmaxf(sc[2][2], sc[2][3])), fmaxf(fmaxf(sc[3][0], sc[3][1]), fmaxf(sc[3][2], sc[3][3]))));
    mx = xmax(mx);
    constexpr float C = 0.0625f * 1.4426950408889634f;
    const float mc = mx * C;
    float l = 0.f;
#pragma unroll
    for (int kt = 0; kt < 4; ++kt)
#pragma unroll
      for (int r = 0; r < 4; ++r) { sc[kt][r] = __builtin_amdgcn_exp2f(fmaf(sc[kt][r], C, -mc)); l += sc[kt][r]; }
    l = xsum(l);
    const float inv = __builtin_amdgcn_rcpf(l);
    const unsigned po = (w >> 1) * 4096 + hh * 1024 + ((w & 1) * 16 + r16 + 32 * blk) * 16;
    const unsigned go = WS_P + team * 65536 + m * 8192 + po;
    plo = lds0 + SX + (m & 3) * 8192 + po;
#pragma unroll
    for (int e = 0; e < 2; ++e) {
      const f32x4 a = sc[2 * e] * inv, c = sc[2 * e + 1] * inv;
      const auto s0 = __builtin_amdgcn_permlane32_swap(pk(a[0], a[1]), pk(c[0], c[1]), false, false);
      const auto s1 = __builtin_amdgcn_permlane32_swap(pk(a[2], a[3]), pk(c[2], c[3]), false, false);
      const u32x4 v = {s0[0], s1[0], s0[1], s1[1]};
      __builtin_amdgcn_raw_buffer_store_b128(v, rs, go + e * 2048, 0, 16);
      pv[e] = v;
    }
  }
  ST(5);
  if (m == 7) {
    asm volatile("s_waitcnt vmcnt(0)" ::: "memory");
    if (lane == 0) __hip_atomic_store(pflag_p + m * 4 + w, want, __ATOMIC_RELAXED, __HIP_MEMORY_SCOPE_AGENT);
    STOUT();
    return;
  }
  const int ct = w & 1, bi = w >> 1;
  f32x16 o[2];
  {
    const unsigned wa = lds0 + SW + (8 * hh + q4) * 128 + (((4 * ct + 2 * blk + (p4 >> 1)) ^ ((q4 >> 1) << 2)) * 16) + (p4 & 1) * 8;
    const unsigned va = lds0 + SW + 16384 + bi * 8192 + r32 * 128 + ((hh ^ ((r32 >> 1) & 7)) * 16);
    auto rd = [&](bf16x8 (&fa)[2][2], bf16x8 (&fb)[2][2], int slot, int half) {
#pragma unroll
      for (int s = 0; s < 2; ++s)
#pragma unroll
        for (int i = 0; i < 2; ++i) {
          const int ks = 2 * half + s;
          const unsigned hi = (slot >> 1) * 65536, lo = (slot & 1) * 32768;
          fa[s][i] = ld128(((va ^ (ks * 32)) + hi) + lo + i * 4096);
          fb[s][i] = ldtr((wa + hi) + lo + i * 8192 + ks * 2048, (wa + hi) + lo + i * 8192 + ks * 2048 + 512);
        }
    };
    auto packC = [&](bf16x8 (&cf)[2][4]) {
#pragma unroll
      for (int e = 0; e < 2; ++e)
#pragma unroll
        for (int s = 0; s < 4; ++s) cf[e][s] = pk16(acc[s >> 1][e], s & 1);
    };
    auto compPC = [&](const bf16x8 (&cf)[2][4], int gl, bool first) {
      const unsigned pa = lds0 + (bi != (m >> 2) ? SW + 3 * 32768 : SX) + gl * 16384 + lane * 16;
#pragma unroll
      for (int e = 0; e < 2; ++e)
#pragma unroll
        for (int s = 0; s < 4; ++s)
#pragma unroll
          for (int qt = 0; qt < 2; ++qt) o[qt] = MFMA32(cf[e][s], ld128(pa + e * 8192 + qt * 4096 + s * 1024), (first && e == 0 && s == 0) ? ZERO16 : o[qt]);
    };
    bf16x8 cf0[2][4], cf1[2][4];
    BAR_C();
    ST(6);
    rd(fa0, fb0, zslot(0), 0);
    *(LAS u32x4*)(size_t)plo = pv[0];
    *(LAS u32x4*)(size_t)(plo + 2048) = pv[1];
#pragma unroll
    for (int z = 0; z < 8; ++z) {
      rd(fa1, fb1, zslot(z), 1);
      mm(fa0, fb0, (z & 3) == 0);
      SG_HALF();
      PIN_ACC();
      if (z == PFLAG_AT) {
        asm volatile("s_waitcnt vmcnt(0)" ::: "memory");
        if (lane == 0) __hip_atomic_store(pflag_p + m * 4 + w, want, __ATOMIC_RELAXED, __HIP_MEMORY_SCOPE_AGENT);
      }
      if (z < 7) {
        BAR_C();
        if (z == 4) ST(8);
        if (z == 5) ST(9);
        if (z == 6) ST(10);
        rd(fa0, fb0, zslot(z + 1), 0);
      }
      mm(fa1, fb1, false);
      if (z < 7) SG_HALF();
      if (z == 3) { packC(cf0); asm volatile("" : "+v"(cf0[0][0]), "+v"(cf0[0][1]), "+v"(cf0[0][2]), "+v"(cf0[0][3]), "+v"(cf0[1][0]), "+v"(cf0[1][1]), "+v"(cf0[1][2]), "+v"(cf0[1][3])); }
      else if (z < 7) PIN_ACC();
      __builtin_amdgcn_sched_barrier(0);
      if (z == 5) {
        compPC(cf0, g1, true);
        SG_DSR(4);
#pragma unroll
        for (int i = 0; i < 12; ++i) { SG_MFMA(1); SG_DSR(1); }
        SG_MFMA(4);
        asm volatile("" : "+v"(o[0]), "+v"(o[1]));
        __builtin_amdgcn_sched_barrier(0);
      }
    }
    packC(cf1);
    compPC(cf1, g1 ^ 1, false);
  }
  ST(11);
  const unsigned fo = WS_F + grp * 65536 + gp * 4096 + w * 1024 + lane * 16;
  GAS unsigned* const fflag_p = (GAS unsigned*)(ws + WS_FFLAG + grp * 256);
#pragma unroll
  for (int k = 0; k < 4; ++k)
    if (gp != k) __builtin_amdgcn_raw_buffer_store_b128(__builtin_bit_cast(u32x4, pk16(o[k >> 1], k & 1)), rs, fo + k * 16384, 0, 16);
  asm volatile("s_waitcnt vmcnt(0)" ::: "memory");
  if (lane == 0) __hip_atomic_store(fflag_p + gp * 4 + w, want, __ATOMIC_RELAXED, __HIP_MEMORY_SCOPE_AGENT);
  ST(12);
  {
    float a[8];
#pragma unroll
    for (int i = 0; i < 8; ++i) a[i] = gp == 0 ? o[0][i] : gp == 1 ? o[0][8 + i] : gp == 2 ? o[1][i] : o[1][8 + i];
    GAS unsigned* const fp = fflag_p + (lane & 3) * 4 + w;
    for (unsigned spins = 0; spins < SPIN_CAP; ++spins) {
      const unsigned fl = __hip_atomic_load(fp, __ATOMIC_RELAXED, __HIP_MEMORY_SCOPE_AGENT);
      if (__all(fl == want || (lane & 3) == gp)) break;
    }
    asm volatile("" ::: "memory");
    ST(13);
    const unsigned fi = WS_F + grp * 65536 + gp * 16384 + w * 1024 + lane * 16;
    u32x4 d[3];
#pragma unroll
    for (int k = 0; k < 3; ++k) d[k] = __builtin_amdgcn_raw_buffer_load_b128(rs, fi + ((gp + 1 + k) & 3) * 4096, 0, 16);
#pragma unroll
    for (int k = 0; k < 3; ++k)
#pragma unroll
      for (int i = 0; i < 4; ++i) { a[2 * i] += __uint_as_float(d[k][i] << 16); a[2 * i + 1] += __uint_as_float(d[k][i] & 0xffff0000u); }
    const auto s0 = __builtin_amdgcn_permlane32_swap(pk(a[0], a[1]), pk(a[4], a[5]), false, false);
    const auto s1 = __builtin_amdgcn_permlane32_swap(pk(a[2], a[3]), pk(a[6], a[7]), false, false);
    char* op = P.out + (((bp * 2 + bi) * 64 + (gp >> 1) * 32 + r32) * 896 + br * 448 + m * 64 + ct * 32 + (gp & 1) * 16 + hh * 8) * 2;
    *(u32x4*)op = u32x4{s0[0], s1[0], s0[1], s1[1]};
    if (gp == 2 && m == 0 && w == 0 && lane == 0) __hip_atomic_store(epoch_p, want, __ATOMIC_RELAXED, __HIP_MEMORY_SCOPE_AGENT);
  }
  ST(14);
  STOUT();
}

extern "C" void kernel_launch(void* const* d_in, const int* in_sizes, int n_in, void* d_out, int out_size,
                              void* d_ws, size_t ws_size, hipStream_t stream) {
  Params p;
  p.x0 = (const char*)d_in[0]; p.k0 = (const char*)d_in[1]; p.v0 = (const char*)d_in[2]; p.wq0 = (const char*)d_in[3]; p.wo0 = (const char*)d_in[4];
  p.x1 = (const char*)d_in[5]; p.k1 = (const char*)d_in[6]; p.v1 = (const char*)d_in[7]; p.wq1 = (const char*)d_in[8]; p.wo1 = (const char*)d_in[9];
  p.out = (char*)d_out; p.ws = (char*)d_ws;
  gqa_fused<<<256, 512, 0, stream>>>(p);
}
```

```cpp
#define STAMP
#include <cstdio>
#include <vector>
#include <algorithm>
#include <unistd.h>
#include <hip/hip_runtime.h>
#include <hip/hip_bf16.h>

typedef short s16x4 __attribute__((ext_vector_type(4)));
typedef short bf16x8 __attribute__((ext_vector_type(8)));
typedef float f32x4 __attribute__((ext_vector_type(4)));
typedef float f32x2 __attribute__((ext_vector_type(2)));
typedef float f32x16 __attribute__((ext_vector_type(16)));
typedef unsigned u32x4 __attribute__((ext_vector_type(4)));
typedef unsigned u32x2 __attribute__((ext_vector_type(2)));
typedef __bf16 bf16x2_t __attribute__((ext_vector_type(2)));
#define LAS __attribute__((address_space(3)))
#define GAS __attribute__((address_space(1)))
#define CAS __attribute__((address_space(4)))
#define DI __device__ __forceinline__

#define ROT_B 2
#define ROT_H 2
#define POL ""
#define PFLAG_AT 1
#define ROT_Z 1

struct Params {
  const char* x0; const char* k0; const char* v0; const char* wq0; const char* wo0;
  const char* x1; const char* k1; const char* v1; const char* wq1; const char* wo1;
  char* out; char* ws;
};

constexpr unsigned WS_EPOCH = 0;
constexpr unsigned WS_PFLAG = 4096;
constexpr unsigned WS_FFLAG = 16384;
constexpr unsigned WS_P = 65536;
constexpr unsigned WS_F = WS_P + 32 * 65536;
constexpr unsigned WS_END = WS_F + 64 * 65536;

constexpr unsigned SW = 0;
constexpr unsigned SX = 131072;

#define STINS "s_memrealtime"
__device__ unsigned long long g_stamp[256 * 2 * 32];
#define ST(i) do { __builtin_amdgcn_sched_barrier(0); asm volatile(STINS " %0\n\ts_waitcnt lgkmcnt(0)" : "=s"(stv[i]) :: "memory"); __builtin_amdgcn_sched_barrier(0); } while (0)
#define STDECL unsigned long long stv[16] = {0,0,0,0,0,0,0,0,0,0,0,0,0,0,0,0}
#define STOUT() do { if (lane == 0 && (w & 3) == 0) { _Pragma("unroll") for (int i_ = 0; i_ < 16; ++i_) g_stamp[(blockIdx.x * 2 + (w >> 2)) * 32 + i_] = stv[i_]; } } while (0)

DI unsigned pk(float a, float b) { f32x2 v = {a, b}; return __builtin_bit_cast(unsigned, __builtin_convertvector(v, bf16x2_t)); }
DI bf16x8 pk8(f32x4 a, f32x4 b) { u32x4 r = {pk(a[0], a[1]), pk(a[2], a[3]), pk(b[0], b[1]), pk(b[2], b[3])}; return __builtin_bit_cast(bf16x8, r); }
DI bf16x8 ld128(unsigned a) { return *(LAS const bf16x8*)(size_t)a; }
DI bf16x8 ldtr(unsigned a0, unsigned a1) {
  s16x4 lo = __builtin_amdgcn_ds_read_tr16_b64_v4i16((LAS s16x4*)(size_t)a0);
  s16x4 hi = __builtin_amdgcn_ds_read_tr16_b64_v4i16((LAS s16x4*)(size_t)a1);
  return __builtin_shufflevector(lo, hi, 0, 1, 2, 3, 4, 5, 6, 7);
}
#define MFMA(a, b, c) __builtin_amdgcn_mfma_f32_16x16x32_bf16((a), (b), (c), 0, 0, 0)
#define MFMA32(a, b, c) __builtin_amdgcn_mfma_f32_32x32x16_bf16((a), (b), (c), 0, 0, 0)
#define ZERO4 (f32x4{0.f, 0.f, 0.f, 0.f})
#define ZERO16 (f32x16{0.f, 0.f, 0.f, 0.f, 0.f, 0.f, 0.f, 0.f, 0.f, 0.f, 0.f, 0.f, 0.f, 0.f, 0.f, 0.f})
DI bf16x8 pk16(const f32x16& x, int a) {
  u32x4 r = {pk(x[8 * a], x[8 * a + 1]), pk(x[8 * a + 2], x[8 * a + 3]), pk(x[8 * a + 4], x[8 * a + 5]), pk(x[8 * a + 6], x[8 * a + 7])};
  return __builtin_bit_cast(bf16x8, r);
}
#define BAR_C() do { __builtin_amdgcn_sched_barrier(0); asm volatile("" ::: "memory"); __builtin_amdgcn_s_waitcnt(0xC07F); \
                     __builtin_amdgcn_s_barrier(); asm volatile("" ::: "memory"); __builtin_amdgcn_sched_barrier(0); } while (0)
#define BAR_L(n) asm volatile("s_waitcnt vmcnt(" #n ")\n\ts_barrier" ::: "memory")
#define SG_MFMA(n) __builtin_amdgcn_sched_group_barrier(0x008, (n), 0)
#define SG_DSR(n) __builtin_amdgcn_sched_group_barrier(0x100, (n), 0)
#define SG_VALU(n) __builtin_amdgcn_sched_group_barrier(0x002, (n), 0)
#define NOLOAD 0
DI void glds(const char* sbase, unsigned voff, unsigned lds_addr) {
  if (NOLOAD) return;
  asm volatile("s_mov_b32 m0, %2\n\ts_nop 0\n\tglobal_load_lds_dwordx4 %0, %1" POL :: "v"(voff), "s"(sbase), "s"(lds_addr) : "memory");
}
DI void glds_dev(const char* sbase, unsigned voff, unsigned lds_addr) {
  asm volatile("s_mov_b32 m0, %2\n\ts_nop 0\n\tglobal_load_lds_dwordx4 %0, %1 sc1" :: "v"(voff), "s"(sbase), "s"(lds_addr) : "memory");
}
DI unsigned next_epoch(unsigned e) { return e * 0x9E3779B1u + 0x7F4A7C15u; }
DI float xmax(float v) {
  auto a = __builtin_amdgcn_permlane32_swap(__float_as_uint(v), __float_as_uint(v), false, false);
  v = fmaxf(__uint_as_float(a[0]), __uint_as_float(a[1]));
  auto b = __builtin_amdgcn_permlane16_swap(__float_as_uint(v), __float_as_uint(v), false, false);
  return fmaxf(__uint_as_float(b[0]), __uint_as_float(b[1]));
}
DI float xsum(float v) {
  auto a = __builtin_amdgcn_permlane32_swap(__float_as_uint(v), __float_as_uint(v), false, false);
  v = __uint_as_float(a[0]) + __uint_as_float(a[1]);
  auto b = __builtin_amdgcn_permlane16_swap(__float_as_uint(v), __float_as_uint(v), false, false);
  return __uint_as_float(b[0]) + __uint_as_float(b[1]);
}
constexpr int zslot(int z) { return (0x10213021 >> (4 * z)) & 3; }
constexpr unsigned SPIN_CAP = 1u << 18;

__global__ __launch_bounds__(512, 2) void gqa_fused(Params P) {
  __shared__ __attribute__((aligned(1024))) char lds[163840];
  const int tid = threadIdx.x, lane = tid & 63;
  const int w = __builtin_amdgcn_readfirstlane(tid >> 6);
  const int bid = blockIdx.x;
  const int xcd = bid & 7, sl = bid >> 3;
  const int br = xcd >> 2, gp = xcd & 3;
  const int bp = sl >> 3, m = sl & 7;
  const int hj = m & 3, h = gp * 4 + hj, b = bp * 2 + (m >> 2);
  const int team = xcd * 4 + bp, comp = br * 4 + bp, grp = comp * 8 + m;
  const unsigned lds0 = (unsigned)(size_t)(LAS char*)lds;
  const int g1 = ROT_Z ? (bp + m) & 1 : 0, k1 = ROT_Z ? (((bp + m) >> 1) & 1) * 2 + (m >> 2) : 0;
  STDECL;
  ST(0);

  if (w >= 4) {
    const int lw = w - 4;
    char* const ws = P.ws;
    GAS unsigned* const epoch_p = (GAS unsigned*)(ws + WS_EPOCH + comp * 256);
    GAS unsigned* const pflag_p = (GAS unsigned*)(ws + WS_PFLAG + team * 256);
    __builtin_amdgcn_s_setprio(3);
    unsigned ep = *(const CAS unsigned*)(size_t)epoch_p;
    const unsigned hi = lane >> 5, l31 = lane & 31;
    const unsigned v_x = (lane >> 3) * 896 + (((lane & 7) ^ (4 * (lw & 1) + (lane >> 4))) * 16);
    const unsigned v_wq = hi * 8192 + ((l31 ^ ((2 * (lw & 1) + hi) << 2)) * 16);
    const unsigned v_k = hi * 512 + ((l31 ^ (2 * lw + hi)) * 16);
    const unsigned v_wo = (lane >> 3) * 896 + (((lane & 7) ^ (((lane >> 4) & 1) << 2)) * 16);
    const unsigned v_v = (lane >> 3) * 512 + (((lane & 7) ^ (4 * (lw & 1) + (lane >> 4))) * 16);
    const unsigned v_k1 = v_k ^ 128;
    const char* const xb = (br ? P.x1 : P.x0) + b * 57344 + lw * (8 * 896);
    const char* const wqb = (br ? P.wq1 : P.wq0) + h * 512 + lw * (2 * 8192);
    const char* const kb = (br ? P.k1 : P.k0) + (b * 8 + (h >> 1)) * 32768 + lw * 1024;
    const char* const vb = (br ? P.v1 : P.v0) + (bp * 16 + gp * 2) * 32768 + lw * 4096;
    const char* const wob = (br ? P.wo1 : P.wo0) + gp * (4 * 229376) + m * 128 + lw * (8 * 896);
    int sa = ((ROT_B == 0 ? 0 : ROT_B == 1 ? b : (b >> 1) * 2) + ROT_H * hj);
    sa = sa >= 7 ? sa - 7 : sa; sa = sa >= 7 ? sa - 7 : sa;
    auto stageA = [&](int slot) {
      const char* wq = wqb + sa * (64 * 8192);
      const unsigned dw = lds0 + SW + slot * 32768 + lw * 1024;
      const char* xs = xb + sa * 128;
      const unsigned dx = lds0 + SX + slot * 8192 + lw * 1024;
      glds(xs, v_x, dx);
      glds(xs + 32 * 896, v_x, dx + 4096);
#pragma unroll
      for (int i = 0; i < 8; ++i) glds(wq + i * (8 * 8192), v_wq, dw + i * 4096);
      sa = sa == 6 ? 0 : sa + 1;
    };
    auto stageKV = [&](const char* src, int slot, int first) {
#pragma unroll
      for (int i = 0; i < 8; ++i) { const int ii = (i + 4 * first) & 7; glds(src + ii * 4096, (i & 1) ? v_k1 : v_k, lds0 + SW + slot * 32768 + lw * 1024 + ii * 4096); }
    };
    auto stageZ = [&](int z, int part) {
      const int gl = g1 ^ (z >> 2), kq = (k1 + z) & 3;
      const char* sw = wob + gl * (2 * 229376) + kq * (64 * 896);
      const char* sv = vb + gl * 32768 + kq * 128;
      const unsigned d = lds0 + SW + zslot(z) * 32768 + lw * 1024;
      if (part & 1) {
#pragma unroll
        for (int i = 0; i < 4; ++i) glds(sw + (i >> 1) * 229376 + (i & 1) * (32 * 896), v_wo, d + i * 4096);
      }
      if (part & 2) {
#pragma unroll
        for (int i = 0; i < 4; ++i) glds(sv + (i >> 1) * (8 * 32768) + (i & 1) * 16384, v_v, d + 16384 + i * 4096);
      }
    };
    stageA(0);
    stageA(1);
    ST(10);
    BAR_L(10); stageA(2);
    asm volatile("" : "+s"(ep));
    ST(1);
#pragma unroll 1
    for (int t = 0; t < 4; ++t) {
      BAR_L(10); stageA(t == 3 ? 0 : t);
    }
    BAR_L(10); stageKV(kb, 3, h & 1);
    if (m == 7) {
      BAR_L(8); BAR_L(63); BAR_L(0);
      STOUT();
      return;
    }
    BAR_L(8); stageZ(0, 3);
    ST(2);
    BAR_L(63);
    BAR_L(8); stageZ(1, 3); stageZ(2, 1);
    ST(3);
    BAR_L(12); stageZ(2, 2); stageZ(3, 1);
    ST(4);
    BAR_L(12); stageZ(3, 2); stageZ(4, 1);
    BAR_L(12); stageZ(4, 2); stageZ(5, 1);
    ST(5);
    unsigned fl;
    const unsigned voff_f = (lane & 31) * 4;
    BAR_L(12);
    asm volatile("global_load_dword %0, %1, %2 sc1" : "=v"(fl) : "v"(voff_f), "s"(pflag_p) : "memory");
    stageZ(5, 2); stageZ(6, 1);
    BAR_L(13); stageZ(6, 2);
    ST(6);
    const unsigned want = next_epoch(ep);
    asm volatile("s_waitcnt vmcnt(12)" : "+v"(fl) :: "memory");
    for (unsigned spins = 0; !__all(fl == want) && spins < SPIN_CAP; ++spins)
      asm volatile("global_load_dword %0, %1, %2 sc1\n\ts_waitcnt vmcnt(0)" : "=v"(fl) : "v"(voff_f), "s"(pflag_p) : "memory");
    ST(7);
    {
      const char* src = ws + WS_P + team * 65536 + lw * 2048;
      const unsigned v_lin = lane * 16;
#pragma unroll 1
      for (int k = 1; k < 8; ++k) {
        const int mm = (m + k) & 7;
        const unsigned dst = lds0 + (((mm ^ m) & 4) ? SW + 3 * 32768 : SX) + (mm & 3) * 8192 + lw * 2048;
        glds_dev(src + mm * 8192, v_lin, dst);
        glds_dev(src + mm * 8192 + 1024, v_lin, dst + 1024);
      }
    }
    BAR_L(22); stageZ(7, 3);
    BAR_L(8);
    ST(8);
    BAR_L(0);
    ST(9);
    STOUT();
    return;
  }

  const unsigned r16 = lane & 15, g = lane >> 4, q4 = r16 >> 2, p4 = lane & 3;
  const unsigned r32 = lane & 31, hh = lane >> 5, blk = g & 1;
  char* const ws = P.ws;
  GAS unsigned* const epoch_p = (GAS unsigned*)(ws + WS_EPOCH + comp * 256);
  GAS unsigned* const pflag_p = (GAS unsigned*)(ws + WS_PFLAG + team * 256);
  const unsigned ep = *(const CAS unsigned*)(size_t)epoch_p;
  const __amdgpu_buffer_rsrc_t rs = __builtin_amdgcn_make_buffer_rsrc(ws, 0, WS_END, 0x00020000);
  f32x16 acc[2][2];
  bf16x8 fa0[2][2], fb0[2][2], fa1[2][2], fb1[2][2];
  auto mm = [&](const bf16x8 (&fa)[2][2], const bf16x8 (&fb)[2][2], bool first) {
#pragma unroll
    for (int s = 0; s < 2; ++s)
#pragma unroll
      for (int i = 0; i < 2; ++i)
#pragma unroll
        for (int j = 0; j < 2; ++j) acc[i][j] = MFMA32(fa[s][i], fb[s][j], (first && s == 0) ? ZERO16 : acc[i][j]);
  };
#define SG_HALF() do { _Pragma("unroll") for (int i_ = 0; i_ < 4; ++i_) { SG_MFMA(1); SG_DSR(3); } SG_MFMA(4); } while (0)
#define PIN_ACC() asm volatile("" : "+v"(acc[0][0]), "+v"(acc[0][1]), "+v"(acc[1][0]), "+v"(acc[1][1]))
  {
    const unsigned xa = lds0 + SX + r32 * 128 + ((hh ^ ((r32 >> 1) & 7)) * 16);
    const unsigned wa = lds0 + SW + (8 * hh + q4) * 512 + (((8 * w + 2 * blk + (p4 >> 1)) ^ (q4 << 2)) * 16) + (p4 & 1) * 8;
    auto rd = [&](bf16x8 (&fa)[2][2], bf16x8 (&fb)[2][2], int slot, int half) {
#pragma unroll
      for (int s = 0; s < 2; ++s)
#pragma unroll
        for (int i = 0; i < 2; ++i) {
          const int ks = 2 * half + s;
          const unsigned wb = (wa ^ (i * 64)) + (slot >> 1) * 65536, wo_ = (slot & 1) * 32768 + ks * 8192;
          fa[s][i] = ldtr(wb + wo_, wb + wo_ + 2048);
          fb[s][i] = ld128((xa ^ (ks * 32)) + slot * 8192 + i * 4096);
        }
    };
    BAR_C();
    ST(1);
    rd(fa0, fb0, 0, 0);
#pragma unroll
    for (int t = 0; t < 7; ++t) {
      rd(fa1, fb1, t % 3, 1);
      mm(fa0, fb0, t == 0);
      SG_HALF();
      PIN_ACC();
      BAR_C();
      if (t < 6) rd(fa0, fb0, (t + 1) % 3, 0);
      mm(fa1, fb1, false);
      if (t < 6) { SG_HALF(); PIN_ACC(); __builtin_amdgcn_sched_barrier(0); }
    }
  }
  ST(2);
  {
    const unsigned qa = lds0 + SX + (r32 >> 4) * 8192 + w * 2048 + r16 * 16 + hh * 8;
#pragma unroll
    for (int i = 0; i < 2; ++i)
#pragma unroll
      for (int j = 0; j < 2; ++j)
#pragma unroll
        for (int a = 0; a < 4; ++a)
          *(LAS u32x2*)(size_t)(qa + j * 16384 + i * 1024 + a * 256) = u32x2{pk(acc[i][j][4 * a], acc[i][j][4 * a + 1]), pk(acc[i][j][4 * a + 2], acc[i][j][4 * a + 3])};
  }
  BAR_C();
  ST(3);
  const unsigned want = next_epoch(ep);
  u32x4 pv[2];
  unsigned plo;
  {
    f32x4 sc[4];
#pragma unroll
    for (int i = 0; i < 4; ++i) sc[i] = ZERO4;
    const unsigned own = lds0 + SX + w * 8192 + lane * 16;
    const unsigned ka = lds0 + SW + 3 * 32768 + r16 * 512 + ((g ^ r16) * 16);
#pragma unroll
    for (int s = 0; s < 8; ++s) {
      const bf16x8 qf = ld128(own + s * 1024);
#pragma unroll
      for (int kt = 0; kt < 4; ++kt) sc[kt] = MFMA(ld128((ka ^ ((s & 3) * 64)) + kt * 8192 + (s >> 2) * 256), qf, sc[kt]);
    }
    SG_VALU(4); SG_DSR(10);
#pragma unroll
    for (int i = 0; i < 30; ++i) { SG_MFMA(1); SG_DSR(1); }
    SG_MFMA(2);
    ST(4);
    float mx = fmaxf(fmaxf(fmaxf(sc[0][0], sc[0][1]), fmaxf(sc[0][2], sc[0][3])), fmaxf(fmaxf(sc[1][0], sc[1][1]), fmaxf(sc[1][2], sc[1][3])));
    mx = fmaxf(mx, fmaxf(fmaxf(fmaxf(sc[2][0], sc[2][1]), fmaxf(sc[2][2], sc[2][3])), fmaxf(fmaxf(sc[3][0], sc[3][1]), fmaxf(sc[3][2], sc[3][3]))));
    mx = xmax(mx);
    constexpr float C = 0.0625f * 1.4426950408889634f;
    const float mc = mx * C;
    float l = 0.f;
#pragma unroll
    for (int kt = 0; kt < 4; ++kt)
#pragma unroll
      for (int r = 0; r < 4; ++r) { sc[kt][r] = __builtin_amdgcn_exp2f(fmaf(sc[kt][r], C, -mc)); l += sc[kt][r]; }
    l = xsum(l);
    const float inv = __builtin_amdgcn_rcpf(l);
    const unsigned po = (w >> 1) * 4096 + hh * 1024 + ((w & 1) * 16 + r16 + 32 * blk) * 16;
    const unsigned go = WS_P + team * 65536 + m * 8192 + po;
    plo = lds0 + SX + (m & 3) * 8192 + po;
#pragma unroll
    for (int e = 0; e < 2; ++e) {
      const f32x4 a = sc[2 * e] * inv, c = sc[2 * e + 1] * inv;
      const auto s0 = __builtin_amdgcn_permlane32_swap(pk(a[0], a[1]), pk(c[0], c[1]), false, false);
      const auto s1 = __builtin_amdgcn_permlane32_swap(pk(a[2], a[3]), pk(c[2], c[3]), false, false);
      const u32x4 v = {s0[0], s1[0], s0[1], s1[1]};
      __builtin_amdgcn_raw_buffer_store_b128(v, rs, go + e * 2048, 0, 16);
      pv[e] = v;
    }
  }
  ST(5);
  if (m == 7) {
    asm volatile("s_waitcnt vmcnt(0)" ::: "memory");
    if (lane == 0) __hip_atomic_store(pflag_p + m * 4 + w, want, __ATOMIC_RELAXED, __HIP_MEMORY_SCOPE_AGENT);
    STOUT();
    return;
  }
  const int ct = w & 1, bi = w >> 1;
  f32x16 o[2];
  {
    const unsigned wa = lds0 + SW + (8 * hh + q4) * 128 + (((4 * ct + 2 * blk + (p4 >> 1)) ^ ((q4 >> 1) << 2)) * 16) + (p4 & 1) * 8;
    const unsigned va = lds0 + SW + 16384 + bi * 8192 + r32 * 128 + ((hh ^ ((r32 >> 1) & 7)) * 16);
    auto rd = [&](bf16x8 (&fa)[2][2], bf16x8 (&fb)[2][2], int slot, int half) {
#pragma unroll
      for (int s = 0; s < 2; ++s)
#pragma unroll
        for (int i = 0; i < 2; ++i) {
          const int ks = 2 * half + s;
          const unsigned hi = (slot >> 1) * 65536, lo = (slot & 1) * 32768;
          fa[s][i] = ld128(((va ^ (ks * 32)) + hi) + lo + i * 4096);
          fb[s][i] = ldtr((wa + hi) + lo + i * 8192 + ks * 2048, (wa + hi) + lo + i * 8192 + ks * 2048 + 512);
        }
    };
    auto packC = [&](bf16x8 (&cf)[2][4]) {
#pragma unroll
      for (int e = 0; e < 2; ++e)
#pragma unroll
        for (int s = 0; s < 4; ++s) cf[e][s] = pk16(acc[s >> 1][e], s & 1);
    };
    auto compPC = [&](const bf16x8 (&cf)[2][4], int gl, bool first) {
      const unsigned pa = lds0 + (bi != (m >> 2) ? SW + 3 * 32768 : SX) + gl * 16384 + lane * 16;
#pragma unroll
      for (int e = 0; e < 2; ++e)
#pragma unroll
        for (int s = 0; s < 4; ++s)
#pragma unroll
          for (int qt = 0; qt < 2; ++qt) o[qt] = MFMA32(cf[e][s], ld128(pa + e * 8192 + qt * 4096 + s * 1024), (first && e == 0 && s == 0) ? ZERO16 : o[qt]);
    };
    bf16x8 cf0[2][4], cf1[2][4];
    BAR_C();
    ST(6);
    rd(fa0, fb0, zslot(0), 0);
    *(LAS u32x4*)(size_t)plo = pv[0];
    *(LAS u32x4*)(size_t)(plo + 2048) = pv[1];
#pragma unroll
    for (int z = 0; z < 8; ++z) {
      rd(fa1, fb1, zslot(z), 1);
      mm(fa0, fb0, (z & 3) == 0);
      SG_HALF();
      PIN_ACC();
      if (z == PFLAG_AT) {
        asm volatile("s_waitcnt vmcnt(0)" ::: "memory");
        if (lane == 0) __hip_atomic_store(pflag_p + m * 4 + w, want, __ATOMIC_RELAXED, __HIP_MEMORY_SCOPE_AGENT);
      }
      if (z < 7) {
        BAR_C();
        if (z == 4) ST(8);
        if (z == 5) ST(9);
        if (z == 6) ST(10);
        rd(fa0, fb0, zslot(z + 1), 0);
      }
      mm(fa1, fb1, false);
      if (z < 7) SG_HALF();
      if (z == 3) { packC(cf0); asm volatile("" : "+v"(cf0[0][0]), "+v"(cf0[0][1]), "+v"(cf0[0][2]), "+v"(cf0[0][3]), "+v"(cf0[1][0]), "+v"(cf0[1][1]), "+v"(cf0[1][2]), "+v"(cf0[1][3])); }
      else if (z < 7) PIN_ACC();
      __builtin_amdgcn_sched_barrier(0);
      if (z == 5) {
        compPC(cf0, g1, true);
        SG_DSR(4);
#pragma unroll
        for (int i = 0; i < 12; ++i) { SG_MFMA(1); SG_DSR(1); }
        SG_MFMA(4);
        asm volatile("" : "+v"(o[0]), "+v"(o[1]));
        __builtin_amdgcn_sched_barrier(0);
      }
    }
    packC(cf1);
    compPC(cf1, g1 ^ 1, false);
  }
  ST(11);
  const unsigned fo = WS_F + grp * 65536 + gp * 4096 + w * 1024 + lane * 16;
  GAS unsigned* const fflag_p = (GAS unsigned*)(ws + WS_FFLAG + grp * 256);
#pragma unroll
  for (int k = 0; k < 4; ++k)
    if (gp != k) __builtin_amdgcn_raw_buffer_store_b128(__builtin_bit_cast(u32x4, pk16(o[k >> 1], k & 1)), rs, fo + k * 16384, 0, 16);
  asm volatile("s_waitcnt vmcnt(0)" ::: "memory");
  if (lane == 0) __hip_atomic_store(fflag_p + gp * 4 + w, want, __ATOMIC_RELAXED, __HIP_MEMORY_SCOPE_AGENT);
  ST(12);
  {
    float a[8];
#pragma unroll
    for (int i = 0; i < 8; ++i) a[i] = gp == 0 ? o[0][i] : gp == 1 ? o[0][8 + i] : gp == 2 ? o[1][i] : o[1][8 + i];
    GAS unsigned* const fp = fflag_p + (lane & 3) * 4 + w;
    for (unsigned spins = 0; spins < SPIN_CAP; ++spins) {
      const unsigned fl = __hip_atomic_load(fp, __ATOMIC_RELAXED, __HIP_MEMORY_SCOPE_AGENT);
      if (__all(fl == want || (lane & 3) == gp)) break;
    }
    asm volatile("" ::: "memory");
    ST(13);
    const unsigned fi = WS_F + grp * 65536 + gp * 16384 + w * 1024 + lane * 16;
    u32x4 d[3];
#pragma unroll
    for (int k = 0; k < 3; ++k) d[k] = __builtin_amdgcn_raw_buffer_load_b128(rs, fi + ((gp + 1 + k) & 3) * 4096, 0, 16);
#pragma unroll
    for (int k = 0; k < 3; ++k)
#pragma unroll
      for (int i = 0; i < 4; ++i) { a[2 * i] += __uint_as_float(d[k][i] << 16); a[2 * i + 1] += __uint_as_float(d[k][i] & 0xffff0000u); }
    const auto s0 = __builtin_amdgcn_permlane32_swap(pk(a[0], a[1]), pk(a[4], a[5]), false, false);
    const auto s1 = __builtin_amdgcn_permlane32_swap(pk(a[2], a[3]), pk(a[6], a[7]), false, false);
    char* op = P.out + (((bp * 2 + bi) * 64 + (gp >> 1) * 32 + r32) * 896 + br * 448 + m * 64 + ct * 32 + (gp & 1) * 16 + hh * 8) * 2;
    *(u32x4*)op = u32x4{s0[0], s1[0], s0[1], s1[1]};
    if (gp == 2 && m == 0 && w == 0 && lane == 0) __hip_atomic_store(epoch_p, want, __ATOMIC_RELAXED, __HIP_MEMORY_SCOPE_AGENT);
  }
  ST(14);
  STOUT();
}

extern "C" void kernel_launch(void* const* d_in, const int* in_sizes, int n_in, void* d_out, int out_size,
                              void* d_ws, size_t ws_size, hipStream_t stream) {
  Params p;
  p.x0 = (const char*)d_in[0]; p.k0 = (const char*)d_in[1]; p.v0 = (const char*)d_in[2]; p.wq0 = (const char*)d_in[3]; p.wo0 = (const char*)d_in[4];
  p.x1 = (const char*)d_in[5]; p.k1 = (const char*)d_in[6]; p.v1 = (const char*)d_in[7]; p.wq1 = (const char*)d_in[8]; p.wo1 = (const char*)d_in[9];
  p.out = (char*)d_out; p.ws = (char*)d_ws;
  gqa_fused<<<256, 512, 0, stream>>>(p);
  static int calls = 0;
  if (++calls == 3) {
    for (int i = 0; i < 10; ++i) fprintf(stderr, "in%d %p ", i, d_in[i]);
    fprintf(stderr, "out %p ws %p\n", d_out, d_ws);
    for (int rep = 0; rep < 4; ++rep) {
      if (rep) { if (rep == 3) { (void)hipMemsetAsync(d_ws, 0x5a, ws_size, stream); (void)hipStreamSynchronize(stream); usleep(50000); } gqa_fused<<<256, 512, 0, stream>>>(p); }
      (void)hipStreamSynchronize(stream);
      std::vector<unsigned long long> st(256 * 2 * 32);
      (void)hipMemcpyFromSymbol(st.data(), HIP_SYMBOL(g_stamp), st.size() * 8);
      unsigned long long t0 = ~0ull; for (int b = 0; b < 512; ++b) t0 = std::min(t0, st[b * 32]);
      for (int role = 0; role < 2; ++role) { fprintf(stderr, "rep%d %s:", rep, role ? "L" : "C");
        for (int i = 0; i < 16; ++i) { std::vector<double> v; for (int b = 0; b < 256; ++b) { unsigned long long x = st[(b * 2 + role) * 32 + i]; if (x >= t0 && x < t0 + 100000) v.push_back((x - t0) * 10.0); }
          if (v.size() < 100) continue; std::sort(v.begin(), v.end()); fprintf(stderr, " %d:%.0f/%.0f", i, v[v.size() / 2], v.back()); }
        fprintf(stderr, "\n"); }
    }
  }
}
```

```cpp
#define WARM_CODE 1
#include <hip/hip_runtime.h>
#include <hip/hip_bf16.h>

typedef short s16x4 __attribute__((ext_vector_type(4)));
typedef short bf16x8 __attribute__((ext_vector_type(8)));
typedef float f32x4 __attribute__((ext_vector_type(4)));
typedef float f32x2 __attribute__((ext_vector_type(2)));
typedef float f32x16 __attribute__((ext_vector_type(16)));
typedef unsigned u32x4 __attribute__((ext_vector_type(4)));
typedef unsigned u32x2 __attribute__((ext_vector_type(2)));
typedef __bf16 bf16x2_t __attribute__((ext_vector_type(2)));
#define LAS __attribute__((address_space(3)))
#define GAS __attribute__((address_space(1)))
#define CAS __attribute__((address_space(4)))
#define DI __device__ __forceinline__

#define ROT_B 2
#define ROT_H 2
#define POL ""
#define PFLAG_AT 1
#define ROT_Z 1

struct Params {
  const char* x0; const char* k0; const char* v0; const char* wq0; const char* wo0;
  const char* x1; const char* k1; const char* v1; const char* wq1; const char* wo1;
  char* out; char* ws;
};

constexpr unsigned WS_EPOCH = 0;
constexpr unsigned WS_PFLAG = 4096;
constexpr unsigned WS_FFLAG = 16384;
constexpr unsigned WS_P = 65536;
constexpr unsigned WS_F = WS_P + 32 * 65536;
constexpr unsigned WS_END = WS_F + 64 * 65536;

constexpr unsigned SW = 0;
constexpr unsigned SX = 131072;

#define ST(i) do {} while (0)
#define STDECL
#define STOUT() do {} while (0)

DI unsigned pk(float a, float b) { f32x2 v = {a, b}; return __builtin_bit_cast(unsigned, __builtin_convertvector(v, bf16x2_t)); }
DI bf16x8 pk8(f32x4 a, f32x4 b) { u32x4 r = {pk(a[0], a[1]), pk(a[2], a[3]), pk(b[0], b[1]), pk(b[2], b[3])}; return __builtin_bit_cast(bf16x8, r); }
DI bf16x8 ld128(unsigned a) { return *(LAS const bf16x8*)(size_t)a; }
DI bf16x8 ldtr(unsigned a0, unsigned a1) {
  s16x4 lo = __builtin_amdgcn_ds_read_tr16_b64_v4i16((LAS s16x4*)(size_t)a0);
  s16x4 hi = __builtin_amdgcn_ds_read_tr16_b64_v4i16((LAS s16x4*)(size_t)a1);
  return __builtin_shufflevector(lo, hi, 0, 1, 2, 3, 4, 5, 6, 7);
}
#define MFMA(a, b, c) __builtin_amdgcn_mfma_f32_16x16x32_bf16((a), (b), (c), 0, 0, 0)
#define MFMA32(a, b, c) __builtin_amdgcn_mfma_f32_32x32x16_bf16((a), (b), (c), 0, 0, 0)
#define ZERO4 (f32x4{0.f, 0.f, 0.f, 0.f})
#define ZERO16 (f32x16{0.f, 0.f, 0.f, 0.f, 0.f, 0.f, 0.f, 0.f, 0.f, 0.f, 0.f, 0.f, 0.f, 0.f, 0.f, 0.f})
DI bf16x8 pk16(const f32x16& x, int a) {
  u32x4 r = {pk(x[8 * a], x[8 * a + 1]), pk(x[8 * a + 2], x[8 * a + 3]), pk(x[8 * a + 4], x[8 * a + 5]), pk(x[8 * a + 6], x[8 * a + 7])};
  return __builtin_bit_cast(bf16x8, r);
}
#define BAR_C() do { __builtin_amdgcn_sched_barrier(0); asm volatile("" ::: "memory"); __builtin_amdgcn_s_waitcnt(0xC07F); \
                     __builtin_amdgcn_s_barrier(); asm volatile("" ::: "memory"); __builtin_amdgcn_sched_barrier(0); } while (0)
#define BAR_L(n) asm volatile("s_waitcnt vmcnt(" #n ")\n\ts_barrier" ::: "memory")
#define SG_MFMA(n) __builtin_amdgcn_sched_group_barrier(0x008, (n), 0)
#define SG_DSR(n) __builtin_amdgcn_sched_group_barrier(0x100, (n), 0)
#define SG_VALU(n) __builtin_amdgcn_sched_group_barrier(0x002, (n), 0)
#define NOLOAD 0
DI void glds(const char* sbase, unsigned voff, unsigned lds_addr) {
  if (NOLOAD) return;
  asm volatile("s_mov_b32 m0, %2\n\ts_nop 0\n\tglobal_load_lds_dwordx4 %0, %1" POL :: "v"(voff), "s"(sbase), "s"(lds_addr) : "memory");
}
DI void glds_dev(const char* sbase, unsigned voff, unsigned lds_addr) {
  asm volatile("s_mov_b32 m0, %2\n\ts_nop 0\n\tglobal_load_lds_dwordx4 %0, %1 sc1" :: "v"(voff), "s"(sbase), "s"(lds_addr) : "memory");
}
DI unsigned next_epoch(unsigned e) { return e * 0x9E3779B1u + 0x7F4A7C15u; }
DI float xmax(float v) {
  auto a = __builtin_amdgcn_permlane32_swap(__float_as_uint(v), __float_as_uint(v), false, false);
  v = fmaxf(__uint_as_float(a[0]), __uint_as_float(a[1]));
  auto b = __builtin_amdgcn_permlane16_swap(__float_as_uint(v), __float_as_uint(v), false, false);
  return fmaxf(__uint_as_float(b[0]), __uint_as_float(b[1]));
}
DI float xsum(float v) {
  auto a = __builtin_amdgcn_permlane32_swap(__float_as_uint(v), __float_as_uint(v), false, false);
  v = __uint_as_float(a[0]) + __uint_as_float(a[1]);
  auto b = __builtin_amdgcn_permlane16_swap(__float_as_uint(v), __float_as_uint(v), false, false);
  return __uint_as_float(b[0]) + __uint_as_float(b[1]);
}
constexpr int zslot(int z) { return (0x10213021 >> (4 * z)) & 3; }
constexpr unsigned SPIN_CAP = 1u << 18;

__global__ __launch_bounds__(512, 2) void gqa_fused(Params P) {
  __shared__ __attribute__((aligned(1024))) char lds[163840];
  const int tid = threadIdx.x, lane = tid & 63;
  const int w = __builtin_amdgcn_readfirstlane(tid >> 6);
  const int bid = blockIdx.x;
  const int xcd = bid & 7, sl = bid >> 3;
  const int br = xcd >> 2, gp = xcd & 3;
  const int bp = sl >> 3, m = sl & 7;
  const int hj = m & 3, h = gp * 4 + hj, b = bp * 2 + (m >> 2);
  const int team = xcd * 4 + bp, comp = br * 4 + bp, grp = comp * 8 + m;
  const unsigned lds0 = (unsigned)(size_t)(LAS char*)lds;
  const int g1 = ROT_Z ? (bp + m) & 1 : 0, k1 = ROT_Z ? (((bp + m) >> 1) & 1) * 2 + (m >> 2) : 0;
  STDECL;
  ST(0);

  if (w >= 4) {
    const int lw = w - 4;
    char* const ws = P.ws;
    GAS unsigned* const epoch_p = (GAS unsigned*)(ws + WS_EPOCH + comp * 256);
    GAS unsigned* const pflag_p = (GAS unsigned*)(ws + WS_PFLAG + team * 256);
    __builtin_amdgcn_s_setprio(3);
    unsigned ep = *(const CAS unsigned*)(size_t)epoch_p;
    const unsigned hi = lane >> 5, l31 = lane & 31;
    const unsigned v_x = (lane >> 3) * 896 + (((lane & 7) ^ (4 * (lw & 1) + (lane >> 4))) * 16);
    const unsigned v_wq = hi * 8192 + ((l31 ^ ((2 * (lw & 1) + hi) << 2)) * 16);
    const unsigned v_k = hi * 512 + ((l31 ^ (2 * lw + hi)) * 16);
    const unsigned v_wo = (lane >> 3) * 896 + (((lane & 7) ^ (((lane >> 4) & 1) << 2)) * 16);
    const unsigned v_v = (lane >> 3) * 512 + (((lane & 7) ^ (4 * (lw & 1) + (lane >> 4))) * 16);
    const unsigned v_k1 = v_k ^ 128;
    const char* const xb = (br ? P.x1 : P.x0) + b * 57344 + lw * (8 * 896);
    const char* const wqb = (br ? P.wq1 : P.wq0) + h * 512 + lw * (2 * 8192);
    const char* const kb = (br ? P.k1 : P.k0) + (b * 8 + (h >> 1)) * 32768 + lw * 1024;
    const char* const vb = (br ? P.v1 : P.v0) + (bp * 16 + gp * 2) * 32768 + lw * 4096;
    const char* const wob = (br ? P.wo1 : P.wo0) + gp * (4 * 229376) + m * 128 + lw * (8 * 896);
    int sa = ((ROT_B == 0 ? 0 : ROT_B == 1 ? b : (b >> 1) * 2) + ROT_H * hj);
    sa = sa >= 7 ? sa - 7 : sa; sa = sa >= 7 ? sa - 7 : sa;
    auto stageA = [&](int slot) {
      const char* wq = wqb + sa * (64 * 8192);
      const unsigned dw = lds0 + SW + slot * 32768 + lw * 1024;
      const char* xs = xb + sa * 128;
      const unsigned dx = lds0 + SX + slot * 8192 + lw * 1024;
      glds(xs, v_x, dx);
      glds(xs + 32 * 896, v_x, dx + 4096);
#pragma unroll
      for (int i = 0; i < 8; ++i) glds(wq + i * (8 * 8192), v_wq, dw + i * 4096);
      sa = sa == 6 ? 0 : sa + 1;
    };
    auto stageKV = [&](const char* src, int slot, int first) {
#pragma unroll
      for (int i = 0; i < 8; ++i) { const int ii = (i + 4 * first) & 7; glds(src + ii * 4096, (i & 1) ? v_k1 : v_k, lds0 + SW + slot * 32768 + lw * 1024 + ii * 4096); }
    };
    auto stageZ = [&](int z, int part) {
      const int gl = g1 ^ (z >> 2), kq = (k1 + z) & 3;
      const char* sw = wob + gl * (2 * 229376) + kq * (64 * 896);
      const char* sv = vb + gl * 32768 + kq * 128;
      const unsigned d = lds0 + SW + zslot(z) * 32768 + lw * 1024;
      if (part & 1) {
#pragma unroll
        for (int i = 0; i < 4; ++i) glds(sw + (i >> 1) * 229376 + (i & 1) * (32 * 896), v_wo, d + i * 4096);
      }
      if (part & 2) {
#pragma unroll
        for (int i = 0; i < 4; ++i) glds(sv + (i >> 1) * (8 * 32768) + (i & 1) * 16384, v_v, d + 16384 + i * 4096);
      }
    };
    stageA(0);
    stageA(1);
    ST(10);
    BAR_L(10); stageA(2);
    asm volatile("" : "+s"(ep));
    ST(1);
#pragma unroll 1
    for (int t = 0; t < 4; ++t) {
      BAR_L(10); stageA(t == 3 ? 0 : t);
    }
    BAR_L(10); stageKV(kb, 3, h & 1);
    if (m == 7) {
      BAR_L(8); BAR_L(63); BAR_L(0);
      STOUT();
      return;
    }
    BAR_L(8); stageZ(0, 3);
    ST(2);
    BAR_L(63);
    BAR_L(8); stageZ(1, 3); stageZ(2, 1);
    ST(3);
    BAR_L(12); stageZ(2, 2); stageZ(3, 1);
    ST(4);
    BAR_L(12); stageZ(3, 2); stageZ(4, 1);
    BAR_L(12); stageZ(4, 2); stageZ(5, 1);
    ST(5);
    unsigned fl;
    const unsigned voff_f = (lane & 31) * 4;
    BAR_L(12);
    asm volatile("global_load_dword %0, %1, %2 sc1" : "=v"(fl) : "v"(voff_f), "s"(pflag_p) : "memory");
    stageZ(5, 2); stageZ(6, 1);
    BAR_L(13); stageZ(6, 2);
    ST(6);
    const unsigned want = next_epoch(ep);
    asm volatile("s_waitcnt vmcnt(12)" : "+v"(fl) :: "memory");
    for (unsigned spins = 0; !__all(fl == want) && spins < SPIN_CAP; ++spins)
      asm volatile("global_load_dword %0, %1, %2 sc1\n\ts_waitcnt vmcnt(0)" : "=v"(fl) : "v"(voff_f), "s"(pflag_p) : "memory");
    ST(7);
    {
      const char* src = ws + WS_P + team * 65536 + lw * 2048;
      const unsigned v_lin = lane * 16;
#pragma unroll 1
      for (int k = 1; k < 8; ++k) {
        const int mm = (m + k) & 7;
        const unsigned dst = lds0 + (((mm ^ m) & 4) ? SW + 3 * 32768 : SX) + (mm & 3) * 8192 + lw * 2048;
        glds_dev(src + mm * 8192, v_lin, dst);
        glds_dev(src + mm * 8192 + 1024, v_lin, dst + 1024);
      }
    }
    BAR_L(22); stageZ(7, 3);
    BAR_L(8);
    ST(8);
    BAR_L(0);
    ST(9);
    STOUT();
    return;
  }

  const unsigned r16 = lane & 15, g = lane >> 4, q4 = r16 >> 2, p4 = lane & 3;
  const unsigned r32 = lane & 31, hh = lane >> 5, blk = g & 1;
  char* const ws = P.ws;
  GAS unsigned* const epoch_p = (GAS unsigned*)(ws + WS_EPOCH + comp * 256);
  GAS unsigned* const pflag_p = (GAS unsigned*)(ws + WS_PFLAG + team * 256);
  const unsigned ep = *(const CAS unsigned*)(size_t)epoch_p;
  const __amdgpu_buffer_rsrc_t rs = __builtin_amdgcn_make_buffer_rsrc(ws, 0, WS_END, 0x00020000);
  {
    unsigned long long pc; asm volatile("s_getpc_b64 %0" : "=s"(pc));
    const char* cp = (const char*)(pc & ~127ull);
    unsigned t0, t1; const unsigned vo = min(lane * 128 + w * 8192, 19328);
    if (w < 3) { asm volatile("global_load_dword %0, %1, %2\n\ts_waitcnt vmcnt(0)" : "=v"(t0) : "v"(vo), "s"(cp) : "memory"); }
  }
  f32x16 acc[2][2];
  bf16x8 fa0[2][2], fb0[2][2], fa1[2][2], fb1[2][2];
  auto mm = [&](const bf16x8 (&fa)[2][2], const bf16x8 (&fb)[2][2], bool first) {
#pragma unroll
    for (int s = 0; s < 2; ++s)
#pragma unroll
      for (int i = 0; i < 2; ++i)
#pragma unroll
        for (int j = 0; j < 2; ++j) acc[i][j] = MFMA32(fa[s][i], fb[s][j], (first && s == 0) ? ZERO16 : acc[i][j]);
  };
#define SG_HALF() do { _Pragma("unroll") for (int i_ = 0; i_ < 4; ++i_) { SG_MFMA(1); SG_DSR(3); } SG_MFMA(4); } while (0)
#define PIN_ACC() asm volatile("" : "+v"(acc[0][0]), "+v"(acc[0][1]), "+v"(acc[1][0]), "+v"(acc[1][1]))
  {
    const unsigned xa = lds0 + SX + r32 * 128 + ((hh ^ ((r32 >> 1) & 7)) * 16);
    const unsigned wa = lds0 + SW + (8 * hh + q4) * 512 + (((8 * w + 2 * blk + (p4 >> 1)) ^ (q4 << 2)) * 16) + (p4 & 1) * 8;
    auto rd = [&](bf16x8 (&fa)[2][2], bf16x8 (&fb)[2][2], int slot, int half) {
#pragma unroll
      for (int s = 0; s < 2; ++s)
#pragma unroll
        for (int i = 0; i < 2; ++i) {
          const int ks = 2 * half + s;
          const unsigned wb = (wa ^ (i * 64)) + (slot >> 1) * 65536, wo_ = (slot & 1) * 32768 + ks * 8192;
          fa[s][i] = ldtr(wb + wo_, wb + wo_ + 2048);
          fb[s][i] = ld128((xa ^ (ks * 32)) + slot * 8192 + i * 4096);
        }
    };
    BAR_C();
    ST(1);
    rd(fa0, fb0, 0, 0);
#pragma unroll
    for (int t = 0; t < 7; ++t) {
      rd(fa1, fb1, t % 3, 1);
      mm(fa0, fb0, t == 0);
      SG_HALF();
      PIN_ACC();
      BAR_C();
      if (t < 6) rd(fa0, fb0, (t + 1) % 3, 0);
      mm(fa1, fb1, false);
      if (t < 6) { SG_HALF(); PIN_ACC(); __builtin_amdgcn_sched_barrier(0); }
    }
  }
  ST(2);
  {
    const unsigned qa = lds0 + SX + (r32 >> 4) * 8192 + w * 2048 + r16 * 16 + hh * 8;
#pragma unroll
    for (int i = 0; i < 2; ++i)
#pragma unroll
      for (int j = 0; j < 2; ++j)
#pragma unroll
        for (int a = 0; a < 4; ++a)
          *(LAS u32x2*)(size_t)(qa + j * 16384 + i * 1024 + a * 256) = u32x2{pk(acc[i][j][4 * a], acc[i][j][4 * a + 1]), pk(acc[i][j][4 * a + 2], acc[i][j][4 * a + 3])};
  }
  BAR_C();
  ST(3);
  const unsigned want = next_epoch(ep);
  u32x4 pv[2];
  unsigned plo;
  {
    f32x4 sc[4];
#pragma unroll
    for (int i = 0; i < 4; ++i) sc[i] = ZERO4;
    const unsigned own = lds0 + SX + w * 8192 + lane * 16;
    const unsigned ka = lds0 + SW + 3 * 32768 + r16 * 512 + ((g ^ r16) * 16);
#pragma unroll
    for (int s = 0; s < 8; ++s) {
      const bf16x8 qf = ld128(own + s * 1024);
#pragma unroll
      for (int kt = 0; kt < 4; ++kt) sc[kt] = MFMA(ld128((ka ^ ((s & 3) * 64)) + kt * 8192 + (s >> 2) * 256), qf, sc[kt]);
    }
    SG_VALU(4); SG_DSR(10);
#pragma unroll
    for (int i = 0; i < 30; ++i) { SG_MFMA(1); SG_DSR(1); }
    SG_MFMA(2);
    ST(4);
    float mx = fmaxf(fmaxf(fmaxf(sc[0][0], sc[0][1]), fmaxf(sc[0][2], sc[0][3])), fmaxf(fmaxf(sc[1][0], sc[1][1]), fmaxf(sc[1][2], sc[1][3])));
    mx = fmaxf(mx, fmaxf(fmaxf(fmaxf(sc[2][0], sc[2][1]), fmaxf(sc[2][2], sc[2][3])), fmaxf(fmaxf(sc[3][0], sc[3][1]), fmaxf(sc[3][2], sc[3][3]))));
    mx = xmax(mx);
    constexpr float C = 0.0625f * 1.4426950408889634f;
    const float mc = mx * C;
    float l = 0.f;
#pragma unroll
    for (int kt = 0; kt < 4; ++kt)
#pragma unroll
      for (int r = 0; r < 4; ++r) { sc[kt][r] = __builtin_amdgcn_exp2f(fmaf(sc[kt][r], C, -mc)); l += sc[kt][r]; }
    l = xsum(l);
    const float inv = __builtin_amdgcn_rcpf(l);
    const unsigned po = (w >> 1) * 4096 + hh * 1024 + ((w & 1) * 16 + r16 + 32 * blk) * 16;
    const unsigned go = WS_P + team * 65536 + m * 8192 + po;
    plo = lds0 + SX + (m & 3) * 8192 + po;
#pragma unroll
    for (int e = 0; e < 2; ++e) {
      const f32x4 a = sc[2 * e] * inv, c = sc[2 * e + 1] * inv;
      const auto s0 = __builtin_amdgcn_permlane32_swap(pk(a[0], a[1]), pk(c[0], c[1]), false, false);
      const auto s1 = __builtin_amdgcn_permlane32_swap(pk(a[2], a[3]), pk(c[2], c[3]), false, false);
      const u32x4 v = {s0[0], s1[0], s0[1], s1[1]};
      __builtin_amdgcn_raw_buffer_store_b128(v, rs, go + e * 2048, 0, 16);
      pv[e] = v;
    }
  }
  ST(5);
  if (m == 7) {
    asm volatile("s_waitcnt vmcnt(0)" ::: "memory");
    if (lane == 0) __hip_atomic_store(pflag_p + m * 4 + w, want, __ATOMIC_RELAXED, __HIP_MEMORY_SCOPE_AGENT);
    STOUT();
    return;
  }
  const int ct = w & 1, bi = w >> 1;
  f32x16 o[2];
  {
    const unsigned wa = lds0 + SW + (8 * hh + q4) * 128 + (((4 * ct + 2 * blk + (p4 >> 1)) ^ ((q4 >> 1) << 2)) * 16) + (p4 & 1) * 8;
    const unsigned va = lds0 + SW + 16384 + bi * 8192 + r32 * 128 + ((hh ^ ((r32 >> 1) & 7)) * 16);
    auto rd = [&](bf16x8 (&fa)[2][2], bf16x8 (&fb)[2][2], int slot, int half) {
#pragma unroll
      for (int s = 0; s < 2; ++s)
#pragma unroll
        for (int i = 0; i < 2; ++i) {
          const int ks = 2 * half + s;
          const unsigned hi = (slot >> 1) * 65536, lo = (slot & 1) * 32768;
          fa[s][i] = ld128(((va ^ (ks * 32)) + hi) + lo + i * 4096);
          fb[s][i] = ldtr((wa + hi) + lo + i * 8192 + ks * 2048, (wa + hi) + lo + i * 8192 + ks * 2048 + 512);
        }
    };
    auto packC = [&](bf16x8 (&cf)[2][4]) {
#pragma unroll
      for (int e = 0; e < 2; ++e)
#pragma unroll
        for (int s = 0; s < 4; ++s) cf[e][s] = pk16(acc[s >> 1][e], s & 1);
    };
    auto compPC = [&](const bf16x8 (&cf)[2][4], int gl, bool first) {
      const unsigned pa = lds0 + (bi != (m >> 2) ? SW + 3 * 32768 : SX) + gl * 16384 + lane * 16;
#pragma unroll
      for (int e = 0; e < 2; ++e)
#pragma unroll
        for (int s = 0; s < 4; ++s)
#pragma unroll
          for (int qt = 0; qt < 2; ++qt) o[qt] = MFMA32(cf[e][s], ld128(pa + e * 8192 + qt * 4096 + s * 1024), (first && e == 0 && s == 0) ? ZERO16 : o[qt]);
    };
    bf16x8 cf0[2][4], cf1[2][4];
    BAR_C();
    ST(6);
    rd(fa0, fb0, zslot(0), 0);
    *(LAS u32x4*)(size_t)plo = pv[0];
    *(LAS u32x4*)(size_t)(plo + 2048) = pv[1];
#pragma unroll
    for (int z = 0; z < 8; ++z) {
      rd(fa1, fb1, zslot(z), 1);
      mm(fa0, fb0, (z & 3) == 0);
      SG_HALF();
      PIN_ACC();
      if (z == PFLAG_AT) {
        asm volatile("s_waitcnt vmcnt(0)" ::: "memory");
        if (lane == 0) __hip_atomic_store(pflag_p + m * 4 + w, want, __ATOMIC_RELAXED, __HIP_MEMORY_SCOPE_AGENT);
      }
      if (z < 7) {
        BAR_C();
        if (z == 4) ST(8);
        if (z == 5) ST(9);
        if (z == 6) ST(10);
        rd(fa0, fb0, zslot(z + 1), 0);
      }
      mm(fa1, fb1, false);
      if (z < 7) SG_HALF();
      if (z == 3) { packC(cf0); asm volatile("" : "+v"(cf0[0][0]), "+v"(cf0[0][1]), "+v"(cf0[0][2]), "+v"(cf0[0][3]), "+v"(cf0[1][0]), "+v"(cf0[1][1]), "+v"(cf0[1][2]), "+v"(cf0[1][3])); }
      else if (z < 7) PIN_ACC();
      __builtin_amdgcn_sched_barrier(0);
      if (z == 5) {
        compPC(cf0, g1, true);
        SG_DSR(4);
#pragma unroll
        for (int i = 0; i < 12; ++i) { SG_MFMA(1); SG_DSR(1); }
        SG_MFMA(4);
        asm volatile("" : "+v"(o[0]), "+v"(o[1]));
        __builtin_amdgcn_sched_barrier(0);
      }
    }
    packC(cf1);
    compPC(cf1, g1 ^ 1, false);
  }
  ST(11);
  const unsigned fo = WS_F + grp * 65536 + gp * 4096 + w * 1024 + lane * 16;
  GAS unsigned* const fflag_p = (GAS unsigned*)(ws + WS_FFLAG + grp * 256);
#pragma unroll
  for (int k = 0; k < 4; ++k)
    if (gp != k) __builtin_amdgcn_raw_buffer_store_b128(__builtin_bit_cast(u32x4, pk16(o[k >> 1], k & 1)), rs, fo + k * 16384, 0, 16);
  asm volatile("s_waitcnt vmcnt(0)" ::: "memory");
  if (lane == 0) __hip_atomic_store(fflag_p + gp * 4 + w, want, __ATOMIC_RELAXED, __HIP_MEMORY_SCOPE_AGENT);
  ST(12);
  {
    float a[8];
#pragma unroll
    for (int i = 0; i < 8; ++i) a[i] = gp == 0 ? o[0][i] : gp == 1 ? o[0][8 + i] : gp == 2 ? o[1][i] : o[1][8 + i];
    GAS unsigned* const fp = fflag_p + (lane & 3) * 4 + w;
    for (unsigned spins = 0; spins < SPIN_CAP; ++spins) {
      const unsigned fl = __hip_atomic_load(fp, __ATOMIC_RELAXED, __HIP_MEMORY_SCOPE_AGENT);
      if (__all(fl == want || (lane & 3) == gp)) break;
    }
    asm volatile("" ::: "memory");
    ST(13);
    const unsigned fi = WS_F + grp * 65536 + gp * 16384 + w * 1024 + lane * 16;
    u32x4 d[3];
#pragma unroll
    for (int k = 0; k < 3; ++k) d[k] = __builtin_amdgcn_raw_buffer_load_b128(rs, fi + ((gp + 1 + k) & 3) * 4096, 0, 16);
#pragma unroll
    for (int k = 0; k < 3; ++k)
#pragma unroll
      for (int i = 0; i < 4; ++i) { a[2 * i] += __uint_as_float(d[k][i] << 16); a[2 * i + 1] += __uint_as_float(d[k][i] & 0xffff0000u); }
    const auto s0 = __builtin_amdgcn_permlane32_swap(pk(a[0], a[1]), pk(a[4], a[5]), false, false);
    const auto s1 = __builtin_amdgcn_permlane32_swap(pk(a[2], a[3]), pk(a[6], a[7]), false, false);
    char* op = P.out + (((bp * 2 + bi) * 64 + (gp >> 1) * 32 + r32) * 896 + br * 448 + m * 64 + ct * 32 + (gp & 1) * 16 + hh * 8) * 2;
    *(u32x4*)op = u32x4{s0[0], s1[0], s0[1], s1[1]};
    if (gp == 2 && m == 0 && w == 0 && lane == 0) __hip_atomic_store(epoch_p, want, __ATOMIC_RELAXED, __HIP_MEMORY_SCOPE_AGENT);
  }
  ST(14);
  STOUT();
}

extern "C" void kernel_launch(void* const* d_in, const int* in_sizes, int n_in, void* d_out, int out_size,
                              void* d_ws, size_t ws_size, hipStream_t stream) {
  Params p;
  p.x0 = (const char*)d_in[0]; p.k0 = (const char*)d_in[1]; p.v0 = (const char*)d_in[2]; p.wq0 = (const char*)d_in[3]; p.wo0 = (const char*)d_in[4];
  p.x1 = (const char*)d_in[5]; p.k1 = (const char*)d_in[6]; p.v1 = (const char*)d_in[7]; p.wq1 = (const char*)d_in[8]; p.wo1 = (const char*)d_in[9];
  p.out = (char*)d_out; p.ws = (char*)d_ws;
  gqa_fused<<<256, 512, 0, stream>>>(p);
}
```

```cpp
#include <hip/hip_runtime.h>
#include <hip/hip_bf16.h>

typedef short s16x4 __attribute__((ext_vector_type(4)));
typedef short bf16x8 __attribute__((ext_vector_type(8)));
typedef float f32x4 __attribute__((ext_vector_type(4)));
typedef float f32x2 __attribute__((ext_vector_type(2)));
typedef float f32x16 __attribute__((ext_vector_type(16)));
typedef unsigned u32x4 __attribute__((ext_vector_type(4)));
typedef unsigned u32x2 __attribute__((ext_vector_type(2)));
typedef __bf16 bf16x2_t __attribute__((ext_vector_type(2)));
#define LAS __attribute__((address_space(3)))
#define GAS __attribute__((address_space(1)))
#define CAS __attribute__((address_space(4)))
#define DI __device__ __forceinline__

#define ROT_B 2
#define ROT_H 2
#define POL ""
#define THR 10
#define P_EARLY 0
#define PFLAG_AT 0
#define ROT_Z 1

struct Params {
  const char* x0; const char* k0; const char* v0; const char* wq0; const char* wo0;
  const char* x1; const char* k1; const char* v1; const char* wq1; const char* wo1;
  char* out; char* ws;
};

constexpr unsigned WS_EPOCH = 0;
constexpr unsigned WS_PFLAG = 4096;
constexpr unsigned WS_FFLAG = 16384;
constexpr unsigned WS_P = 65536;
constexpr unsigned WS_F = WS_P + 32 * 65536;
constexpr unsigned WS_END = WS_F + 64 * 65536;

constexpr unsigned SW = 0;
constexpr unsigned SX = 131072;

#define ST(i) do {} while (0)
#define STDECL
#define STOUT() do {} while (0)

DI unsigned pk(float a, float b) { f32x2 v = {a, b}; return __builtin_bit_cast(unsigned, __builtin_convertvector(v, bf16x2_t)); }
DI bf16x8 pk8(f32x4 a, f32x4 b) { u32x4 r = {pk(a[0], a[1]), pk(a[2], a[3]), pk(b[0], b[1]), pk(b[2], b[3])}; return __builtin_bit_cast(bf16x8, r); }
DI bf16x8 ld128(unsigned a) { return *(LAS const bf16x8*)(size_t)a; }
DI bf16x8 ldtr(unsigned a0, unsigned a1) {
  s16x4 lo = __builtin_amdgcn_ds_read_tr16_b64_v4i16((LAS s16x4*)(size_t)a0);
  s16x4 hi = __builtin_amdgcn_ds_read_tr16_b64_v4i16((LAS s16x4*)(size_t)a1);
  return __builtin_shufflevector(lo, hi, 0, 1, 2, 3, 4, 5, 6, 7);
}
#define MFMA(a, b, c) __builtin_amdgcn_mfma_f32_16x16x32_bf16((a), (b), (c), 0, 0, 0)
#define MFMA32(a, b, c) __builtin_amdgcn_mfma_f32_32x32x16_bf16((a), (b), (c), 0, 0, 0)
#define ZERO4 (f32x4{0.f, 0.f, 0.f, 0.f})
#define ZERO16 (f32x16{0.f, 0.f, 0.f, 0.f, 0.f, 0.f, 0.f, 0.f, 0.f, 0.f, 0.f, 0.f, 0.f, 0.f, 0.f, 0.f})
DI bf16x8 pk16(const f32x16& x, int a) {
  u32x4 r = {pk(x[8 * a], x[8 * a + 1]), pk(x[8 * a + 2], x[8 * a + 3]), pk(x[8 * a + 4], x[8 * a + 5]), pk(x[8 * a + 6], x[8 * a + 7])};
  return __builtin_bit_cast(bf16x8, r);
}
#define BAR_C() do { __builtin_amdgcn_sched_barrier(0); asm volatile("" ::: "memory"); __builtin_amdgcn_s_waitcnt(0xC07F); \
                     __builtin_amdgcn_s_barrier(); asm volatile("" ::: "memory"); __builtin_amdgcn_sched_barrier(0); } while (0)
#define BAR_L(n) asm volatile("s_waitcnt vmcnt(" #n ")\n\ts_barrier" ::: "memory")
#define SG_MFMA(n) __builtin_amdgcn_sched_group_barrier(0x008, (n), 0)
#define SG_DSR(n) __builtin_amdgcn_sched_group_barrier(0x100, (n), 0)
#define SG_VALU(n) __builtin_amdgcn_sched_group_barrier(0x002, (n), 0)
#define NOLOAD 0
DI void glds(const char* sbase, unsigned voff, unsigned lds_addr) {
  if (NOLOAD) return;
  asm volatile("s_mov_b32 m0, %2\n\ts_nop 0\n\tglobal_load_lds_dwordx4 %0, %1" POL "\n\ts_waitcnt vmcnt(%3)" :: "v"(voff), "s"(sbase), "s"(lds_addr), "i"(THR) : "memory");
}
DI void glds_dev(const char* sbase, unsigned voff, unsigned lds_addr) {
  asm volatile("s_mov_b32 m0, %2\n\ts_nop 0\n\tglobal_load_lds_dwordx4 %0, %1 sc1\n\ts_waitcnt vmcnt(%3)" :: "v"(voff), "s"(sbase), "s"(lds_addr), "i"(THR) : "memory");
}
DI unsigned next_epoch(unsigned e) { return e * 0x9E3779B1u + 0x7F4A7C15u; }
DI float xmax(float v) {
  auto a = __builtin_amdgcn_permlane32_swap(__float_as_uint(v), __float_as_uint(v), false, false);
  v = fmaxf(__uint_as_float(a[0]), __uint_as_float(a[1]));
  auto b = __builtin_amdgcn_permlane16_swap(__float_as_uint(v), __float_as_uint(v), false, false);
  return fmaxf(__uint_as_float(b[0]), __uint_as_float(b[1]));
}
DI float xsum(float v) {
  auto a = __builtin_amdgcn_permlane32_swap(__float_as_uint(v), __float_as_uint(v), false, false);
  v = __uint_as_float(a[0]) + __uint_as_float(a[1]);
  auto b = __builtin_amdgcn_permlane16_swap(__float_as_uint(v), __float_as_uint(v), false, false);
  return __uint_as_float(b[0]) + __uint_as_float(b[1]);
}
constexpr int zslot(int z) { return (0x10213021 >> (4 * z)) & 3; }
constexpr unsigned SPIN_CAP = 1u << 18;

__global__ __launch_bounds__(512, 2) void gqa_fused(Params P) {
  __shared__ __attribute__((aligned(1024))) char lds[163840];
  const int tid = threadIdx.x, lane = tid & 63;
  const int w = __builtin_amdgcn_readfirstlane(tid >> 6);
  const int bid = blockIdx.x;
  const int xcd = bid & 7, sl = bid >> 3;
  const int br = xcd >> 2, gp = xcd & 3;
  const int bp = sl >> 3, m = sl & 7;
  const int hj = m & 3, h = gp * 4 + hj, b = bp * 2 + (m >> 2);
  const int team = xcd * 4 + bp, comp = br * 4 + bp, grp = comp * 8 + m;
  const unsigned lds0 = (unsigned)(size_t)(LAS char*)lds;
  const int g1 = ROT_Z ? (bp + m) & 1 : 0, k1 = ROT_Z ? (((bp + m) >> 1) & 1) * 2 + (m >> 2) : 0;
  STDECL;
  ST(0);

  if (w >= 4) {
    const int lw = w - 4;
    char* const ws = P.ws;
    GAS unsigned* const epoch_p = (GAS unsigned*)(ws + WS_EPOCH + comp * 256);
    GAS unsigned* const pflag_p = (GAS unsigned*)(ws + WS_PFLAG + team * 256);
    __builtin_amdgcn_s_setprio(3);
    unsigned ep = *(const CAS unsigned*)(size_t)epoch_p;
    const unsigned hi = lane >> 5, l31 = lane & 31;
    const unsigned v_x = (lane >> 3) * 896 + (((lane & 7) ^ (4 * (lw & 1) + (lane >> 4))) * 16);
    const unsigned v_wq = hi * 8192 + ((l31 ^ ((2 * (lw & 1) + hi) << 2)) * 16);
    const unsigned v_k = hi * 512 + ((l31 ^ (2 * lw + hi)) * 16);
    const unsigned v_wo = (lane >> 3) * 896 + (((lane & 7) ^ (((lane >> 4) & 1) << 2)) * 16);
    const unsigned v_v = (lane >> 3) * 512 + (((lane & 7) ^ (4 * (lw & 1) + (lane >> 4))) * 16);
    const unsigned v_k1 = v_k ^ 128;
    const char* const xb = (br ? P.x1 : P.x0) + b * 57344 + lw * (8 * 896);
    const char* const wqb = (br ? P.wq1 : P.wq0) + h * 512 + lw * (2 * 8192);
    const char* const kb = (br ? P.k1 : P.k0) + (b * 8 + (h >> 1)) * 32768 + lw * 1024;
    const char* const vb = (br ? P.v1 : P.v0) + (bp * 16 + gp * 2) * 32768 + lw * 4096;
    const char* const wob = (br ? P.wo1 : P.wo0) + gp * (4 * 229376) + m * 128 + lw * (8 * 896);
    int sa = ((ROT_B == 0 ? 0 : ROT_B == 1 ? b : (b >> 1) * 2) + ROT_H * hj);
    sa = sa >= 7 ? sa - 7 : sa; sa = sa >= 7 ? sa - 7 : sa;
    auto stageA = [&](int slot) {
      const char* wq = wqb + sa * (64 * 8192);
      const unsigned dw = lds0 + SW + slot * 32768 + lw * 1024;
      const char* xs = xb + sa * 128;
      const unsigned dx = lds0 + SX + slot * 8192 + lw * 1024;
      glds(xs, v_x, dx);
      glds(xs + 32 * 896, v_x, dx + 4096);
#pragma unroll
      for (int i = 0; i < 8; ++i) glds(wq + i * (8 * 8192), v_wq, dw + i * 4096);
      sa = sa == 6 ? 0 : sa + 1;
    };
    auto stageKV = [&](const char* src, int slot, int first) {
#pragma unroll
      for (int i = 0; i < 8; ++i) { const int ii = (i + 4 * first) & 7; glds(src + ii * 4096, (i & 1) ? v_k1 : v_k, lds0 + SW + slot * 32768 + lw * 1024 + ii * 4096); }
    };
    auto stageZ = [&](int z, int part) {
      const int gl = g1 ^ (z >> 2), kq = (k1 + z) & 3;
      const char* sw = wob + gl * (2 * 229376) + kq * (64 * 896);
      const char* sv = vb + gl * 32768 + kq * 128;
      const unsigned d = lds0 + SW + zslot(z) * 32768 + lw * 1024;
      if (part & 1) {
#pragma unroll
        for (int i = 0; i < 4; ++i) glds(sw + (i >> 1) * 229376 + (i & 1) * (32 * 896), v_wo, d + i * 4096);
      }
      if (part & 2) {
#pragma unroll
        for (int i = 0; i < 4; ++i) glds(sv + (i >> 1) * (8 * 32768) + (i & 1) * 16384, v_v, d + 16384 + i * 4096);
      }
    };
    stageA(0);
    stageA(1);
    ST(10);
    BAR_L(10); stageA(2);
    asm volatile("" : "+s"(ep));
    ST(1);
#pragma unroll 1
    for (int t = 0; t < 4; ++t) {
      BAR_L(10); stageA(t == 3 ? 0 : t);
    }
    BAR_L(10); stageKV(kb, 3, h & 1);
    if (m == 7) {
      BAR_L(8); BAR_L(63); BAR_L(0);
      STOUT();
      return;
    }
    BAR_L(8); stageZ(0, 3);
    ST(2);
    BAR_L(63);
    BAR_L(8); stageZ(1, 3); stageZ(2, 1);
    ST(3);
    BAR_L(12); stageZ(2, 2); stageZ(3, 1);
    ST(4);
    BAR_L(12); ST(11); stageZ(3, 2); stageZ(4, 1);
    BAR_L(12); ST(12); stageZ(4, 2); stageZ(5, 1);
    ST(5);
    unsigned fl;
    const unsigned voff_f = (lane & 31) * 4;
    const unsigned want = next_epoch(ep);
    auto stageP = [&](int k0, int k1_) {
      const char* src = ws + WS_P + team * 65536 + lw * 2048;
      const unsigned v_lin = lane * 16;
#pragma unroll 1
      for (int k = k0; k < k1_; ++k) {
        const int mm = ((m + k) & 3) | ((m ^ k) & 4);
        const unsigned dst = lds0 + ((k & 4) ? SW + 3 * 32768 : SX) + (mm & 3) * 8192 + lw * 2048;
        glds_dev(src + mm * 8192, v_lin, dst);
        glds_dev(src + mm * 8192 + 1024, v_lin, dst + 1024);
      }
    };
    BAR_L(12); ST(13); stageZ(5, 2);
    asm volatile("global_load_dword %0, %1, %2 sc1" : "=v"(fl) : "v"(voff_f), "s"(pflag_p) : "memory");
    stageZ(6, 1);
    BAR_L(13); ST(14); stageZ(6, 2);
    asm volatile("s_waitcnt vmcnt(8)" : "+v"(fl) :: "memory");
    for (unsigned spins = 0; !__all(fl == want) && spins < SPIN_CAP; ++spins)
      asm volatile("global_load_dword %0, %1, %2 sc1\n\ts_waitcnt vmcnt(0)" : "=v"(fl) : "v"(voff_f), "s"(pflag_p) : "memory");
    ST(7);
    stageP(1, 4);
    BAR_L(14); ST(15); stageP(4, 8);
    BAR_L(14); stageZ(7, 3);
    ST(8);
    BAR_L(8);
    BAR_L(0);
    ST(9);
    STOUT();
    return;
  }

  const unsigned r16 = lane & 15, g = lane >> 4, q4 = r16 >> 2, p4 = lane & 3;
  const unsigned r32 = lane & 31, hh = lane >> 5, blk = g & 1;
  char* const ws = P.ws;
  GAS unsigned* const epoch_p = (GAS unsigned*)(ws + WS_EPOCH + comp * 256);
  GAS unsigned* const pflag_p = (GAS unsigned*)(ws + WS_PFLAG + team * 256);
  const unsigned ep = *(const CAS unsigned*)(size_t)epoch_p;
  const __amdgpu_buffer_rsrc_t rs = __builtin_amdgcn_make_buffer_rsrc(ws, 0, WS_END, 0x00020000);
  f32x16 acc[2][2];
  bf16x8 fa0[2][2], fb0[2][2], fa1[2][2], fb1[2][2];
  auto mm = [&](const bf16x8 (&fa)[2][2], const bf16x8 (&fb)[2][2], bool first) {
#pragma unroll
    for (int s = 0; s < 2; ++s)
#pragma unroll
      for (int i = 0; i < 2; ++i)
#pragma unroll
        for (int j = 0; j < 2; ++j) acc[i][j] = MFMA32(fa[s][i], fb[s][j], (first && s == 0) ? ZERO16 : acc[i][j]);
  };
#define SG_HALF() do { _Pragma("unroll") for (int i_ = 0; i_ < 4; ++i_) { SG_MFMA(1); SG_DSR(3); } SG_MFMA(4); } while (0)
#define PIN_ACC() asm volatile("" : "+v"(acc[0][0]), "+v"(acc[0][1]), "+v"(acc[1][0]), "+v"(acc[1][1]))
  {
    const unsigned xa = lds0 + SX + r32 * 128 + ((hh ^ ((r32 >> 1) & 7)) * 16);
    const unsigned wa = lds0 + SW + (8 * hh + q4) * 512 + (((8 * w + 2 * blk + (p4 >> 1)) ^ (q4 << 2)) * 16) + (p4 & 1) * 8;
    auto rd = [&](bf16x8 (&fa)[2][2], bf16x8 (&fb)[2][2], int slot, int half) {
#pragma unroll
      for (int s = 0; s < 2; ++s)
#pragma unroll
        for (int i = 0; i < 2; ++i) {
          const int ks = 2 * half + s;
          const unsigned wb = (wa ^ (i * 64)) + (slot >> 1) * 65536, wo_ = (slot & 1) * 32768 + ks * 8192;
          fa[s][i] = ldtr(wb + wo_, wb + wo_ + 2048);
          fb[s][i] = ld128((xa ^ (ks * 32)) + slot * 8192 + i * 4096);
        }
    };
    BAR_C();
    ST(1);
    rd(fa0, fb0, 0, 0);
#pragma unroll
    for (int t = 0; t < 7; ++t) {
      rd(fa1, fb1, t % 3, 1);
      mm(fa0, fb0, t == 0);
      SG_HALF();
      PIN_ACC();
      BAR_C();
      if (t < 6) rd(fa0, fb0, (t + 1) % 3, 0);
      mm(fa1, fb1, false);
      if (t < 6) { SG_HALF(); PIN_ACC(); __builtin_amdgcn_sched_barrier(0); }
    }
  }
  ST(2);
  {
    const unsigned qa = lds0 + SX + (r32 >> 4) * 8192 + w * 2048 + r16 * 16 + hh * 8;
#pragma unroll
    for (int i = 0; i < 2; ++i)
#pragma unroll
      for (int j = 0; j < 2; ++j)
#pragma unroll
        for (int a = 0; a < 4; ++a)
          *(LAS u32x2*)(size_t)(qa + j * 16384 + i * 1024 + a * 256) = u32x2{pk(acc[i][j][4 * a], acc[i][j][4 * a + 1]), pk(acc[i][j][4 * a + 2], acc[i][j][4 * a + 3])};
  }
  BAR_C();
  ST(3);
  const unsigned want = next_epoch(ep);
  u32x4 pv[2];
  unsigned plo;
  {
    f32x4 sc[4];
#pragma unroll
    for (int i = 0; i < 4; ++i) sc[i] = ZERO4;
    const unsigned own = lds0 + SX + w * 8192 + lane * 16;
    const unsigned ka = lds0 + SW + 3 * 32768 + r16 * 512 + ((g ^ r16) * 16);
#pragma unroll
    for (int s = 0; s < 8; ++s) {
      const bf16x8 qf = ld128(own + s * 1024);
#pragma unroll
      for (int kt = 0; kt < 4; ++kt) sc[kt] = MFMA(ld128((ka ^ ((s & 3) * 64)) + kt * 8192 + (s >> 2) * 256), qf, sc[kt]);
    }
    SG_VALU(4); SG_DSR(10);
#pragma unroll
    for (int i = 0; i < 30; ++i) { SG_MFMA(1); SG_DSR(1); }
    SG_MFMA(2);
    ST(4);
    float mx = fmaxf(fmaxf(fmaxf(sc[0][0], sc[0][1]), fmaxf(sc[0][2], sc[0][3])), fmaxf(fmaxf(sc[1][0], sc[1][1]), fmaxf(sc[1][2], sc[1][3])));
    mx = fmaxf(mx, fmaxf(fmaxf(fmaxf(sc[2][0], sc[2][1]), fmaxf(sc[2][2], sc[2][3])), fmaxf(fmaxf(sc[3][0], sc[3][1]), fmaxf(sc[3][2], sc[3][3]))));
    mx = xmax(mx);
    constexpr float C = 0.0625f * 1.4426950408889634f;
    const float mc = mx * C;
    float l = 0.f;
#pragma unroll
    for (int kt = 0; kt < 4; ++kt)
#pragma unroll
      for (int r = 0; r < 4; ++r) { sc[kt][r] = __builtin_amdgcn_exp2f(fmaf(sc[kt][r], C, -mc)); l += sc[kt][r]; }
    l = xsum(l);
    const float inv = __builtin_amdgcn_rcpf(l);
    const unsigned po = (w >> 1) * 4096 + hh * 1024 + ((w & 1) * 16 + r16 + 32 * blk) * 16;
    const unsigned go = WS_P + team * 65536 + m * 8192 + po;
    plo = lds0 + SX + (m & 3) * 8192 + po;
#pragma unroll
    for (int e = 0; e < 2; ++e) {
      const f32x4 a = sc[2 * e] * inv, c = sc[2 * e + 1] * inv;
      const auto s0 = __builtin_amdgcn_permlane32_swap(pk(a[0], a[1]), pk(c[0], c[1]), false, false);
      const auto s1 = __builtin_amdgcn_permlane32_swap(pk(a[2], a[3]), pk(c[2], c[3]), false, false);
      const u32x4 v = {s0[0], s1[0], s0[1], s1[1]};
      __builtin_amdgcn_raw_buffer_store_b128(v, rs, go + e * 2048, 0, 16);
      pv[e] = v;
    }
  }
  ST(5);
  if (m == 7) {
    asm volatile("s_waitcnt vmcnt(0)" ::: "memory");
    if (lane == 0) __hip_atomic_store(pflag_p + m * 4 + w, want, __ATOMIC_RELAXED, __HIP_MEMORY_SCOPE_AGENT);
    STOUT();
    return;
  }
  const int ct = w & 1, bi = w >> 1;
  f32x16 o[2];
  {
    const unsigned wa = lds0 + SW + (8 * hh + q4) * 128 + (((4 * ct + 2 * blk + (p4 >> 1)) ^ ((q4 >> 1) << 2)) * 16) + (p4 & 1) * 8;
    const unsigned va = lds0 + SW + 16384 + bi * 8192 + r32 * 128 + ((hh ^ ((r32 >> 1) & 7)) * 16);
    auto rd = [&](bf16x8 (&fa)[2][2], bf16x8 (&fb)[2][2], int slot, int half) {
#pragma unroll
      for (int s = 0; s < 2; ++s)
#pragma unroll
        for (int i = 0; i < 2; ++i) {
          const int ks = 2 * half + s;
          const unsigned hi = (slot >> 1) * 65536, lo = (slot & 1) * 32768;
          fa[s][i] = ld128(((va ^ (ks * 32)) + hi) + lo + i * 4096);
          fb[s][i] = ldtr((wa + hi) + lo + i * 8192 + ks * 2048, (wa + hi) + lo + i * 8192 + ks * 2048 + 512);
        }
    };
    auto packC = [&](bf16x8 (&cf)[2][4]) {
#pragma unroll
      for (int e = 0; e < 2; ++e)
#pragma unroll
        for (int s = 0; s < 4; ++s) cf[e][s] = pk16(acc[s >> 1][e], s & 1);
    };
    auto compPC = [&](const bf16x8 (&cf)[2][4], int gl, bool first) {
      const unsigned pa = lds0 + (bi != (m >> 2) ? SW + 3 * 32768 : SX) + gl * 16384 + lane * 16;
#pragma unroll
      for (int e = 0; e < 2; ++e)
#pragma unroll
        for (int s = 0; s < 4; ++s)
#pragma unroll
          for (int qt = 0; qt < 2; ++qt) o[qt] = MFMA32(cf[e][s], ld128(pa + e * 8192 + qt * 4096 + s * 1024), (first && e == 0 && s == 0) ? ZERO16 : o[qt]);
    };
    bf16x8 cf0[2][4], cf1[2][4];
    BAR_C();
    ST(6);
    rd(fa0, fb0, zslot(0), 0);
    *(LAS u32x4*)(size_t)plo = pv[0];
    *(LAS u32x4*)(size_t)(plo + 2048) = pv[1];
#pragma unroll
    for (int z = 0; z < 8; ++z) {
      rd(fa1, fb1, zslot(z), 1);
      mm(fa0, fb0, (z & 3) == 0);
      SG_HALF();
      PIN_ACC();
      if (z == PFLAG_AT) {
        asm volatile("s_waitcnt vmcnt(0)" ::: "memory");
        if (lane == 0) __hip_atomic_store(pflag_p + m * 4 + w, want, __ATOMIC_RELAXED, __HIP_MEMORY_SCOPE_AGENT);
      }
      if (z == 6) {
        BAR_C();
        compPC(cf0, g1, true);
        SG_DSR(4);
#pragma unroll
        for (int i = 0; i < 12; ++i) { SG_MFMA(1); SG_DSR(1); }
        SG_MFMA(4);
        asm volatile("" : "+v"(o[0]), "+v"(o[1]));
      }
      if (z < 7) {
        BAR_C();
        if (z == 4) ST(8);
        if (z == 5) ST(9);
        if (z == 6) ST(10);
        rd(fa0, fb0, zslot(z + 1), 0);
      }
      mm(fa1, fb1, false);
      if (z < 7) SG_HALF();
      if (z == 3) { packC(cf0); asm volatile("" : "+v"(cf0[0][0]), "+v"(cf0[0][1]), "+v"(cf0[0][2]), "+v"(cf0[0][3]), "+v"(cf0[1][0]), "+v"(cf0[1][1]), "+v"(cf0[1][2]), "+v"(cf0[1][3])); }
      else if (z < 7) PIN_ACC();
      __builtin_amdgcn_sched_barrier(0);
    }
    packC(cf1);
    compPC(cf1, g1 ^ 1, false);
  }
  ST(11);
  const unsigned fo = WS_F + grp * 65536 + gp * 4096 + w * 1024 + lane * 16;
  GAS unsigned* const fflag_p = (GAS unsigned*)(ws + WS_FFLAG + grp * 256);
#pragma unroll
  for (int k = 0; k < 4; ++k)
    if (gp != k) __builtin_amdgcn_raw_buffer_store_b128(__builtin_bit_cast(u32x4, pk16(o[k >> 1], k & 1)), rs, fo + k * 16384, 0, 16);
  asm volatile("s_waitcnt vmcnt(0)" ::: "memory");
  if (lane == 0) __hip_atomic_store(fflag_p + gp * 4 + w, want, __ATOMIC_RELAXED, __HIP_MEMORY_SCOPE_AGENT);
  ST(12);
  {
    float a[8];
#pragma unroll
    for (int i = 0; i < 8; ++i) a[i] = gp == 0 ? o[0][i] : gp == 1 ? o[0][8 + i] : gp == 2 ? o[1][i] : o[1][8 + i];
    GAS unsigned* const fp = fflag_p + (lane & 3) * 4 + w;
    for (unsigned spins = 0; spins < SPIN_CAP; ++spins) {
      const unsigned fl = __hip_atomic_load(fp, __ATOMIC_RELAXED, __HIP_MEMORY_SCOPE_AGENT);
      if (__all(fl == want || (lane & 3) == gp)) break;
    }
    asm volatile("" ::: "memory");
    ST(13);
    const unsigned fi = WS_F + grp * 65536 + gp * 16384 + w * 1024 + lane * 16;
    u32x4 d[3];
#pragma unroll
    for (int k = 0; k < 3; ++k) d[k] = __builtin_amdgcn_raw_buffer_load_b128(rs, fi + ((gp + 1 + k) & 3) * 4096, 0, 16);
#pragma unroll
    for (int k = 0; k < 3; ++k)
#pragma unroll
      for (int i = 0; i < 4; ++i) { a[2 * i] += __uint_as_float(d[k][i] << 16); a[2 * i + 1] += __uint_as_float(d[k][i] & 0xffff0000u); }
    const auto s0 = __builtin_amdgcn_permlane32_swap(pk(a[0], a[1]), pk(a[4], a[5]), false, false);
    const auto s1 = __builtin_amdgcn_permlane32_swap(pk(a[2], a[3]), pk(a[6], a[7]), false, false);
    char* op = P.out + (((bp * 2 + bi) * 64 + (gp >> 1) * 32 + r32) * 896 + br * 448 + m * 64 + ct * 32 + (gp & 1) * 16 + hh * 8) * 2;
    *(u32x4*)op = u32x4{s0[0], s1[0], s0[1], s1[1]};
    if (gp == 2 && m == 0 && w == 0 && lane == 0) __hip_atomic_store(epoch_p, want, __ATOMIC_RELAXED, __HIP_MEMORY_SCOPE_AGENT);
  }
  ST(14);
  STOUT();
}

extern "C" void kernel_launch(void* const* d_in, const int* in_sizes, int n_in, void* d_out, int out_size,
                              void* d_ws, size_t ws_size, hipStream_t stream) {
  Params p;
  p.x0 = (const char*)d_in[0]; p.k0 = (const char*)d_in[1]; p.v0 = (const char*)d_in[2]; p.wq0 = (const char*)d_in[3]; p.wo0 = (const char*)d_in[4];
  p.x1 = (const char*)d_in[5]; p.k1 = (const char*)d_in[6]; p.v1 = (const char*)d_in[7]; p.wq1 = (const char*)d_in[8]; p.wo1 = (const char*)d_in[9];
  p.out = (char*)d_out; p.ws = (char*)d_ws;
  gqa_fused<<<256, 512, 0, stream>>>(p);
}
```

```cpp
#include <hip/hip_runtime.h>
#include <hip/hip_bf16.h>

typedef short s16x4 __attribute__((ext_vector_type(4)));
typedef short bf16x8 __attribute__((ext_vector_type(8)));
typedef float f32x4 __attribute__((ext_vector_type(4)));
typedef float f32x2 __attribute__((ext_vector_type(2)));
typedef float f32x16 __attribute__((ext_vector_type(16)));
typedef unsigned u32x4 __attribute__((ext_vector_type(4)));
typedef unsigned u32x2 __attribute__((ext_vector_type(2)));
typedef unsigned u32x16 __attribute__((ext_vector_type(16)));
typedef unsigned u32x8 __attribute__((ext_vector_type(8)));
typedef __bf16 bf16x2_t __attribute__((ext_vector_type(2)));
#define LAS __attribute__((address_space(3)))
#define GAS __attribute__((address_space(1)))
#define CAS __attribute__((address_space(4)))
#define DI __device__ __forceinline__

#define ROT_B 2
#define ROT_H 2
#define POL ""
#define THR 10
#define P_EARLY 0
#define PFLAG_AT 0
#define ROT_Z 1

struct Params {
  const char* x0; const char* k0; const char* v0; const char* wq0; const char* wo0;
  const char* x1; const char* k1; const char* v1; const char* wq1; const char* wo1;
  char* out; char* ws;
};

constexpr unsigned WS_EPOCH = 0;
constexpr unsigned WS_PFLAG = 4096;
constexpr unsigned WS_FFLAG = 16384;
constexpr unsigned WS_P = 65536;
constexpr unsigned WS_F = WS_P + 32 * 65536;
constexpr unsigned WS_END = WS_F + 64 * 65536;

constexpr unsigned SW = 0;
constexpr unsigned SX = 131072;

#define ST(i) do {} while (0)
#define STDECL
#define STOUT() do {} while (0)

DI unsigned pk(float a, float b) { f32x2 v = {a, b}; return __builtin_bit_cast(unsigned, __builtin_convertvector(v, bf16x2_t)); }
DI bf16x8 pk8(f32x4 a, f32x4 b) { u32x4 r = {pk(a[0], a[1]), pk(a[2], a[3]), pk(b[0], b[1]), pk(b[2], b[3])}; return __builtin_bit_cast(bf16x8, r); }
DI bf16x8 ld128(unsigned a) { return *(LAS const bf16x8*)(size_t)a; }
DI bf16x8 ldtr(unsigned a0, unsigned a1) {
  s16x4 lo = __builtin_amdgcn_ds_read_tr16_b64_v4i16((LAS s16x4*)(size_t)a0);
  s16x4 hi = __builtin_amdgcn_ds_read_tr16_b64_v4i16((LAS s16x4*)(size_t)a1);
  return __builtin_shufflevector(lo, hi, 0, 1, 2, 3, 4, 5, 6, 7);
}
#define MFMA(a, b, c) __builtin_amdgcn_mfma_f32_16x16x32_bf16((a), (b), (c), 0, 0, 0)
#define MFMA32(a, b, c) __builtin_amdgcn_mfma_f32_32x32x16_bf16((a), (b), (c), 0, 0, 0)
#define ZERO4 (f32x4{0.f, 0.f, 0.f, 0.f})
#define ZERO16 (f32x16{0.f, 0.f, 0.f, 0.f, 0.f, 0.f, 0.f, 0.f, 0.f, 0.f, 0.f, 0.f, 0.f, 0.f, 0.f, 0.f})
DI bf16x8 pk16(const f32x16& x, int a) {
  u32x4 r = {pk(x[8 * a], x[8 * a + 1]), pk(x[8 * a + 2], x[8 * a + 3]), pk(x[8 * a + 4], x[8 * a + 5]), pk(x[8 * a + 6], x[8 * a + 7])};
  return __builtin_bit_cast(bf16x8, r);
}
#define BAR_C() do { __builtin_amdgcn_sched_barrier(0); asm volatile("" ::: "memory"); __builtin_amdgcn_s_waitcnt(0xC07F); \
                     __builtin_amdgcn_s_barrier(); asm volatile("" ::: "memory"); __builtin_amdgcn_sched_barrier(0); } while (0)
#define BAR_L(n) asm volatile("s_waitcnt vmcnt(" #n ")\n\ts_barrier" ::: "memory")
#define SG_MFMA(n) __builtin_amdgcn_sched_group_barrier(0x008, (n), 0)
#define SG_DSR(n) __builtin_amdgcn_sched_group_barrier(0x100, (n), 0)
#define SG_VALU(n) __builtin_amdgcn_sched_group_barrier(0x002, (n), 0)
#define NOLOAD 0
DI void glds(const char* sbase, unsigned voff, unsigned lds_addr) {
  if (NOLOAD) return;
  asm volatile("s_mov_b32 m0, %2\n\ts_nop 0\n\tglobal_load_lds_dwordx4 %0, %1" POL "\n\ts_waitcnt vmcnt(%3)" :: "v"(voff), "s"(sbase), "s"(lds_addr), "i"(THR) : "memory");
}
DI void glds_dev(const char* sbase, unsigned voff, unsigned lds_addr) {
  asm volatile("s_mov_b32 m0, %2\n\ts_nop 0\n\tglobal_load_lds_dwordx4 %0, %1 sc1\n\ts_waitcnt vmcnt(%3)" :: "v"(voff), "s"(sbase), "s"(lds_addr), "i"(THR) : "memory");
}
DI unsigned next_epoch(unsigned e) { return e * 0x9E3779B1u + 0x7F4A7C15u; }
DI float xmax(float v) {
  auto a = __builtin_amdgcn_permlane32_swap(__float_as_uint(v), __float_as_uint(v), false, false);
  v = fmaxf(__uint_as_float(a[0]), __uint_as_float(a[1]));
  auto b = __builtin_amdgcn_permlane16_swap(__float_as_uint(v), __float_as_uint(v), false, false);
  return fmaxf(__uint_as_float(b[0]), __uint_as_float(b[1]));
}
DI float xsum(float v) {
  auto a = __builtin_amdgcn_permlane32_swap(__float_as_uint(v), __float_as_uint(v), false, false);
  v = __uint_as_float(a[0]) + __uint_as_float(a[1]);
  auto b = __builtin_amdgcn_permlane16_swap(__float_as_uint(v), __float_as_uint(v), false, false);
  return __uint_as_float(b[0]) + __uint_as_float(b[1]);
}
constexpr int zslot(int z) { return (0x10213021 >> (4 * z)) & 3; }
constexpr int CODE_BYTES = 21504;
constexpr unsigned SPIN_CAP = 1u << 18;

__global__ __launch_bounds__(512, 2) void gqa_fused(Params P) {
  __shared__ __attribute__((aligned(1024))) char lds[163840];
  u32x16 ka; u32x8 kb8;
  asm volatile("s_load_dwordx16 %0, %2, 0x0\n\ts_load_dwordx8 %1, %2, 0x40" : "=&s"(ka), "=&s"(kb8) : "s"(__builtin_amdgcn_kernarg_segment_ptr()));
  const int tid = threadIdx.x, lane = tid & 63;
  const int w = __builtin_amdgcn_readfirstlane(tid >> 6);
  const int bid = blockIdx.x;
  const int xcd = bid & 7, sl = bid >> 3;
  const int br = xcd >> 2, gp = xcd & 3;
  const int bp = sl >> 3, m = sl & 7;
  const int hj = m & 3, h = gp * 4 + hj, b = bp * 2 + (m >> 2);
  const int team = xcd * 4 + bp, comp = br * 4 + bp, grp = comp * 8 + m;
  const unsigned lds0 = (unsigned)(size_t)(LAS char*)lds;
  const int g1 = ROT_Z ? (bp + m) & 1 : 0, k1 = ROT_Z ? (((bp + m) >> 1) & 1) * 2 + (m >> 2) : 0;
  STDECL;
  ST(0);
  {
    unsigned long long pc; asm volatile("s_getpc_b64 %0" : "=s"(pc));
    const unsigned vo = min((sl * 8 + w) * 128, CODE_BYTES - 128);
    asm volatile("s_mov_b32 m0, %2\n\ts_nop 0\n\tglobal_load_lds_dword %0, %1" :: "v"(vo), "s"(pc & ~127ull), "s"(lds0 + SX + 3 * 8192 + w * 256) : "memory");
  }
  const int lw = w & 3;
  const unsigned hi = lane >> 5, l31 = lane & 31;
  unsigned v_x = (lane >> 3) * 896 + (((lane & 7) ^ (4 * (lw & 1) + (lane >> 4))) * 16);
  unsigned v_wq = hi * 8192 + ((l31 ^ ((2 * (lw & 1) + hi) << 2)) * 16);
  int sa = ((ROT_B == 0 ? 0 : ROT_B == 1 ? b : (b >> 1) * 2) + ROT_H * hj);
  sa = sa >= 7 ? sa - 7 : sa; sa = sa >= 7 ? sa - 7 : sa;
  int ox = b * 57344 + lw * (8 * 896), owq = h * 512 + lw * (2 * 8192);
  asm volatile("" : "+v"(v_x), "+v"(v_wq));
  asm volatile("s_waitcnt lgkmcnt(0)" : "+s"(ka), "+s"(kb8), "+s"(sa), "+s"(ox), "+s"(owq));
#define KPTR(lo, hi) ((char*)(((unsigned long long)(hi) << 32) | (lo)))
#define K_X (br ? KPTR(ka[10], ka[11]) : KPTR(ka[0], ka[1]))
#define K_K (br ? KPTR(ka[12], ka[13]) : KPTR(ka[2], ka[3]))
#define K_V (br ? KPTR(ka[14], ka[15]) : KPTR(ka[4], ka[5]))
#define K_WQ (br ? KPTR(kb8[0], kb8[1]) : KPTR(ka[6], ka[7]))
#define K_WO (br ? KPTR(kb8[2], kb8[3]) : KPTR(ka[8], ka[9]))
#define K_OUT KPTR(kb8[4], kb8[5])
#define K_WS KPTR(kb8[6], kb8[7])

  if (__builtin_expect(w >= 4, 1)) {
    __builtin_amdgcn_s_setprio(3);
    const char* const xb = K_X + ox;
    const char* const wqb = K_WQ + owq;
    auto stageA = [&](int slot) {
      const char* wq = wqb + sa * (64 * 8192);
      const unsigned dw = lds0 + SW + slot * 32768 + lw * 1024;
      const char* xs = xb + sa * 128;
      const unsigned dx = lds0 + SX + slot * 8192 + lw * 1024;
      glds(xs, v_x, dx);
      glds(xs + 32 * 896, v_x, dx + 4096);
#pragma unroll
      for (int i = 0; i < 8; ++i) glds(wq + i * (8 * 8192), v_wq, dw + i * 4096);
      sa = sa == 6 ? 0 : sa + 1;
    };
    stageA(0);
    char* const ws = K_WS;
    GAS unsigned* const epoch_p = (GAS unsigned*)(ws + WS_EPOCH + comp * 256);
    GAS unsigned* const pflag_p = (GAS unsigned*)(ws + WS_PFLAG + team * 256);
    unsigned ep = *(const CAS unsigned*)(size_t)epoch_p;
    const unsigned v_k = hi * 512 + ((l31 ^ (2 * lw + hi)) * 16);
    const unsigned v_wo = (lane >> 3) * 896 + (((lane & 7) ^ (((lane >> 4) & 1) << 2)) * 16);
    const unsigned v_v = (lane >> 3) * 512 + (((lane & 7) ^ (4 * (lw & 1) + (lane >> 4))) * 16);
    const unsigned v_k1 = v_k ^ 128;
    const char* const kb = K_K + (b * 8 + (h >> 1)) * 32768 + lw * 1024;
    const char* const vb = K_V + (bp * 16 + gp * 2) * 32768 + lw * 4096;
    const char* const wob = K_WO + gp * (4 * 229376) + m * 128 + lw * (8 * 896);
    auto stageKV = [&](const char* src, int slot, int first) {
#pragma unroll
      for (int i = 0; i < 8; ++i) { const int ii = (i + 4 * first) & 7; glds(src + ii * 4096, (i & 1) ? v_k1 : v_k, lds0 + SW + slot * 32768 + lw * 1024 + ii * 4096); }
    };
    auto stageZ = [&](int z, int part) {
      const int gl = g1 ^ (z >> 2), kq = (k1 + z) & 3;
      const char* sw = wob + gl * (2 * 229376) + kq * (64 * 896);
      const char* sv = vb + gl * 32768 + kq * 128;
      const unsigned d = lds0 + SW + zslot(z) * 32768 + lw * 1024;
      if (part & 1) {
#pragma unroll
        for (int i = 0; i < 4; ++i) glds(sw + (i >> 1) * 229376 + (i & 1) * (32 * 896), v_wo, d + i * 4096);
      }
      if (part & 2) {
#pragma unroll
        for (int i = 0; i < 4; ++i) glds(sv + (i >> 1) * (8 * 32768) + (i & 1) * 16384, v_v, d + 16384 + i * 4096);
      }
    };
    stageA(1);
    ST(10);
    BAR_L(10); stageA(2);
    asm volatile("" : "+s"(ep));
    ST(1);
#pragma unroll 1
    for (int t = 0; t < 4; ++t) {
      BAR_L(10); stageA(t == 3 ? 0 : t);
    }
    BAR_L(10); stageKV(kb, 3, h & 1);
    if (m == 7) {
      BAR_L(8); BAR_L(63); BAR_L(0);
      STOUT();
      return;
    }
    BAR_L(8); stageZ(0, 3);
    ST(2);
    BAR_L(63);
    BAR_L(8); stageZ(1, 3); stageZ(2, 1);
    ST(3);
    BAR_L(12); stageZ(2, 2); stageZ(3, 1);
    ST(4);
    BAR_L(12); ST(11); stageZ(3, 2); stageZ(4, 1);
    BAR_L(12); ST(12); stageZ(4, 2); stageZ(5, 1);
    ST(5);
    unsigned fl;
    const unsigned voff_f = (lane & 31) * 4;
    const unsigned want = next_epoch(ep);
    auto stageP = [&](int k0, int k1_) {
      const char* src = ws + WS_P + team * 65536 + lw * 2048;
      const unsigned v_lin = lane * 16;
#pragma unroll 1
      for (int k = k0; k < k1_; ++k) {
        const int mm = ((m + k) & 3) | ((m ^ k) & 4);
        const unsigned dst = lds0 + ((k & 4) ? SW + 3 * 32768 : SX) + (mm & 3) * 8192 + lw * 2048;
        glds_dev(src + mm * 8192, v_lin, dst);
        glds_dev(src + mm * 8192 + 1024, v_lin, dst + 1024);
      }
    };
    BAR_L(12); ST(13); stageZ(5, 2);
    asm volatile("global_load_dword %0, %1, %2 sc1" : "=v"(fl) : "v"(voff_f), "s"(pflag_p) : "memory");
    stageZ(6, 1);
    BAR_L(13); ST(14); stageZ(6, 2);
    asm volatile("s_waitcnt vmcnt(8)" : "+v"(fl) :: "memory");
    for (unsigned spins = 0; !__all(fl == want) && spins < SPIN_CAP; ++spins)
      asm volatile("global_load_dword %0, %1, %2 sc1\n\ts_waitcnt vmcnt(0)" : "=v"(fl) : "v"(voff_f), "s"(pflag_p) : "memory");
    ST(7);
    stageP(1, 4);
    BAR_L(14); ST(15); stageP(4, 8);
    BAR_L(14); stageZ(7, 3);
    ST(8);
    BAR_L(8);
    BAR_L(0);
    ST(9);
    STOUT();
    return;
  }

  const unsigned r16 = lane & 15, g = lane >> 4, q4 = r16 >> 2, p4 = lane & 3;
  const unsigned r32 = lane & 31, hh = lane >> 5, blk = g & 1;
  asm volatile("s_waitcnt vmcnt(0)" ::: "memory");
  char* const ws = K_WS;
  GAS unsigned* const epoch_p = (GAS unsigned*)(ws + WS_EPOCH + comp * 256);
  GAS unsigned* const pflag_p = (GAS unsigned*)(ws + WS_PFLAG + team * 256);
  const unsigned ep = *(const CAS unsigned*)(size_t)epoch_p;
  const __amdgpu_buffer_rsrc_t rs = __builtin_amdgcn_make_buffer_rsrc(ws, 0, WS_END, 0x00020000);
  f32x16 acc[2][2];
  bf16x8 fa0[2][2], fb0[2][2], fa1[2][2], fb1[2][2];
  auto mm = [&](const bf16x8 (&fa)[2][2], const bf16x8 (&fb)[2][2], bool first) {
#pragma unroll
    for (int s = 0; s < 2; ++s)
#pragma unroll
      for (int i = 0; i < 2; ++i)
#pragma unroll
        for (int j = 0; j < 2; ++j) acc[i][j] = MFMA32(fa[s][i], fb[s][j], (first && s == 0) ? ZERO16 : acc[i][j]);
  };
#define SG_HALF() do { _Pragma("unroll") for (int i_ = 0; i_ < 4; ++i_) { SG_MFMA(1); SG_DSR(3); } SG_MFMA(4); } while (0)
#define PIN_ACC() asm volatile("" : "+v"(acc[0][0]), "+v"(acc[0][1]), "+v"(acc[1][0]), "+v"(acc[1][1]))
  {
    const unsigned xa = lds0 + SX + r32 * 128 + ((hh ^ ((r32 >> 1) & 7)) * 16);
    const unsigned wa = lds0 + SW + (8 * hh + q4) * 512 + (((8 * w + 2 * blk + (p4 >> 1)) ^ (q4 << 2)) * 16) + (p4 & 1) * 8;
    auto rd = [&](bf16x8 (&fa)[2][2], bf16x8 (&fb)[2][2], int slot, int half) {
#pragma unroll
      for (int s = 0; s < 2; ++s)
#pragma unroll
        for (int i = 0; i < 2; ++i) {
          const int ks = 2 * half + s;
          const unsigned wb = (wa ^ (i * 64)) + (slot >> 1) * 65536, wo_ = (slot & 1) * 32768 + ks * 8192;
          fa[s][i] = ldtr(wb + wo_, wb + wo_ + 2048);
          fb[s][i] = ld128((xa ^ (ks * 32)) + slot * 8192 + i * 4096);
        }
    };
    BAR_C();
    ST(1);
    rd(fa0, fb0, 0, 0);
#pragma unroll
    for (int t = 0; t < 7; ++t) {
      rd(fa1, fb1, t % 3, 1);
      mm(fa0, fb0, t == 0);
      SG_HALF();
      PIN_ACC();
      BAR_C();
      if (t < 6) rd(fa0, fb0, (t + 1) % 3, 0);
      mm(fa1, fb1, false);
      if (t < 6) { SG_HALF(); PIN_ACC(); __builtin_amdgcn_sched_barrier(0); }
    }
  }
  ST(2);
  {
    const unsigned qa = lds0 + SX + (r32 >> 4) * 8192 + w * 2048 + r16 * 16 + hh * 8;
#pragma unroll
    for (int i = 0; i < 2; ++i)
#pragma unroll
      for (int j = 0; j < 2; ++j)
#pragma unroll
        for (int a = 0; a < 4; ++a)
          *(LAS u32x2*)(size_t)(qa + j * 16384 + i * 1024 + a * 256) = u32x2{pk(acc[i][j][4 * a], acc[i][j][4 * a + 1]), pk(acc[i][j][4 * a + 2], acc[i][j][4 * a + 3])};
  }
  BAR_C();
  ST(3);
  const unsigned want = next_epoch(ep);
  u32x4 pv[2];
  unsigned plo;
  {
    f32x4 sc[4];
#pragma unroll
    for (int i = 0; i < 4; ++i) sc[i] = ZERO4;
    const unsigned own = lds0 + SX + w * 8192 + lane * 16;
    const unsigned ka = lds0 + SW + 3 * 32768 + r16 * 512 + ((g ^ r16) * 16);
#pragma unroll
    for (int s = 0; s < 8; ++s) {
      const bf16x8 qf = ld128(own + s * 1024);
#pragma unroll
      for (int kt = 0; kt < 4; ++kt) sc[kt] = MFMA(ld128((ka ^ ((s & 3) * 64)) + kt * 8192 + (s >> 2) * 256), qf, sc[kt]);
    }
    SG_VALU(4); SG_DSR(10);
#pragma unroll
    for (int i = 0; i < 30; ++i) { SG_MFMA(1); SG_DSR(1); }
    SG_MFMA(2);
    ST(4);
    float mx = fmaxf(fmaxf(fmaxf(sc[0][0], sc[0][1]), fmaxf(sc[0][2], sc[0][3])), fmaxf(fmaxf(sc[1][0], sc[1][1]), fmaxf(sc[1][2], sc[1][3])));
    mx = fmaxf(mx, fmaxf(fmaxf(fmaxf(sc[2][0], sc[2][1]), fmaxf(sc[2][2], sc[2][3])), fmaxf(fmaxf(sc[3][0], sc[3][1]), fmaxf(sc[3][2], sc[3][3]))));
    mx = xmax(mx);
    constexpr float C = 0.0625f * 1.4426950408889634f;
    const float mc = mx * C;
    float l = 0.f;
#pragma unroll
    for (int kt = 0; kt < 4; ++kt)
#pragma unroll
      for (int r = 0; r < 4; ++r) { sc[kt][r] = __builtin_amdgcn_exp2f(fmaf(sc[kt][r], C, -mc)); l += sc[kt][r]; }
    l = xsum(l);
    const float inv = __builtin_amdgcn_rcpf(l);
    const unsigned po = (w >> 1) * 4096 + hh * 1024 + ((w & 1) * 16 + r16 + 32 * blk) * 16;
    const unsigned go = WS_P + team * 65536 + m * 8192 + po;
    plo = lds0 + SX + (m & 3) * 8192 + po;
#pragma unroll
    for (int e = 0; e < 2; ++e) {
      const f32x4 a = sc[2 * e] * inv, c = sc[2 * e + 1] * inv;
      const auto s0 = __builtin_amdgcn_permlane32_swap(pk(a[0], a[1]), pk(c[0], c[1]), false, false);
      const auto s1 = __builtin_amdgcn_permlane32_swap(pk(a[2], a[3]), pk(c[2], c[3]), false, false);
      const u32x4 v = {s0[0], s1[0], s0[1], s1[1]};
      __builtin_amdgcn_raw_buffer_store_b128(v, rs, go + e * 2048, 0, 16);
      pv[e] = v;
    }
  }
  ST(5);
  if (m == 7) {
    asm volatile("s_waitcnt vmcnt(0)" ::: "memory");
    if (lane == 0) __hip_atomic_store(pflag_p + m * 4 + w, want, __ATOMIC_RELAXED, __HIP_MEMORY_SCOPE_AGENT);
    STOUT();
    return;
  }
  const int ct = w & 1, bi = w >> 1;
  f32x16 o[2];
  {
    const unsigned wa = lds0 + SW + (8 * hh + q4) * 128 + (((4 * ct + 2 * blk + (p4 >> 1)) ^ ((q4 >> 1) << 2)) * 16) + (p4 & 1) * 8;
    const unsigned va = lds0 + SW + 16384 + bi * 8192 + r32 * 128 + ((hh ^ ((r32 >> 1) & 7)) * 16);
    auto rd = [&](bf16x8 (&fa)[2][2], bf16x8 (&fb)[2][2], int slot, int half) {
#pragma unroll
      for (int s = 0; s < 2; ++s)
#pragma unroll
        for (int i = 0; i < 2; ++i) {
          const int ks = 2 * half + s;
          const unsigned hi = (slot >> 1) * 65536, lo = (slot & 1) * 32768;
          fa[s][i] = ld128(((va ^ (ks * 32)) + hi) + lo + i * 4096);
          fb[s][i] = ldtr((wa + hi) + lo + i * 8192 + ks * 2048, (wa + hi) + lo + i * 8192 + ks * 2048 + 512);
        }
    };
    auto packC = [&](bf16x8 (&cf)[2][4]) {
#pragma unroll
      for (int e = 0; e < 2; ++e)
#pragma unroll
        for (int s = 0; s < 4; ++s) cf[e][s] = pk16(acc[s >> 1][e], s & 1);
    };
    auto compPC = [&](const bf16x8 (&cf)[2][4], int gl, bool first) {
      const unsigned pa = lds0 + (bi != (m >> 2) ? SW + 3 * 32768 : SX) + gl * 16384 + lane * 16;
#pragma unroll
      for (int e = 0; e < 2; ++e)
#pragma unroll
        for (int s = 0; s < 4; ++s)
#pragma unroll
          for (int qt = 0; qt < 2; ++qt) o[qt] = MFMA32(cf[e][s], ld128(pa + e * 8192 + qt * 4096 + s * 1024), (first && e == 0 && s == 0) ? ZERO16 : o[qt]);
    };
    bf16x8 cf0[2][4], cf1[2][4];
    BAR_C();
    ST(6);
    rd(fa0, fb0, zslot(0), 0);
    *(LAS u32x4*)(size_t)plo = pv[0];
    *(LAS u32x4*)(size_t)(plo + 2048) = pv[1];
#pragma unroll
    for (int z = 0; z < 8; ++z) {
      rd(fa1, fb1, zslot(z), 1);
      mm(fa0, fb0, (z & 3) == 0);
      SG_HALF();
      PIN_ACC();
      if (z == PFLAG_AT) {
        asm volatile("s_waitcnt vmcnt(0)" ::: "memory");
        if (lane == 0) __hip_atomic_store(pflag_p + m * 4 + w, want, __ATOMIC_RELAXED, __HIP_MEMORY_SCOPE_AGENT);
      }
      if (z == 6) {
        BAR_C();
        compPC(cf0, g1, true);
        SG_DSR(4);
#pragma unroll
        for (int i = 0; i < 12; ++i) { SG_MFMA(1); SG_DSR(1); }
        SG_MFMA(4);
        asm volatile("" : "+v"(o[0]), "+v"(o[1]));
      }
      if (z < 7) {
        BAR_C();
        if (z == 4) ST(8);
        if (z == 5) ST(9);
        if (z == 6) ST(10);
        rd(fa0, fb0, zslot(z + 1), 0);
      }
      mm(fa1, fb1, false);
      if (z < 7) SG_HALF();
      if (z == 3) { packC(cf0); asm volatile("" : "+v"(cf0[0][0]), "+v"(cf0[0][1]), "+v"(cf0[0][2]), "+v"(cf0[0][3]), "+v"(cf0[1][0]), "+v"(cf0[1][1]), "+v"(cf0[1][2]), "+v"(cf0[1][3])); }
      else if (z < 7) PIN_ACC();
      __builtin_amdgcn_sched_barrier(0);
    }
    packC(cf1);
    compPC(cf1, g1 ^ 1, false);
  }
  ST(11);
  const unsigned fo = WS_F + grp * 65536 + gp * 4096 + w * 1024 + lane * 16;
  GAS unsigned* const fflag_p = (GAS unsigned*)(ws + WS_FFLAG + grp * 256);
#pragma unroll
  for (int k = 0; k < 4; ++k)
    if (gp != k) __builtin_amdgcn_raw_buffer_store_b128(__builtin_bit_cast(u32x4, pk16(o[k >> 1], k & 1)), rs, fo + k * 16384, 0, 16);
  asm volatile("s_waitcnt vmcnt(0)" ::: "memory");
  if (lane == 0) __hip_atomic_store(fflag_p + gp * 4 + w, want, __ATOMIC_RELAXED, __HIP_MEMORY_SCOPE_AGENT);
  ST(12);
  {
    float a[8];
#pragma unroll
    for (int i = 0; i < 8; ++i) a[i] = gp == 0 ? o[0][i] : gp == 1 ? o[0][8 + i] : gp == 2 ? o[1][i] : o[1][8 + i];
    GAS unsigned* const fp = fflag_p + (lane & 3) * 4 + w;
    for (unsigned spins = 0; spins < SPIN_CAP; ++spins) {
      const unsigned fl = __hip_atomic_load(fp, __ATOMIC_RELAXED, __HIP_MEMORY_SCOPE_AGENT);
      if (__all(fl == want || (lane & 3) == gp)) break;
    }
    asm volatile("" ::: "memory");
    ST(13);
    const unsigned fi = WS_F + grp * 65536 + gp * 16384 + w * 1024 + lane * 16;
    u32x4 d[3];
#pragma unroll
    for (int k = 0; k < 3; ++k) d[k] = __builtin_amdgcn_raw_buffer_load_b128(rs, fi + ((gp + 1 + k) & 3) * 4096, 0, 16);
#pragma unroll
    for (int k = 0; k < 3; ++k)
#pragma unroll
      for (int i = 0; i < 4; ++i) { a[2 * i] += __uint_as_float(d[k][i] << 16); a[2 * i + 1] += __uint_as_float(d[k][i] & 0xffff0000u); }
    const auto s0 = __builtin_amdgcn_permlane32_swap(pk(a[0], a[1]), pk(a[4], a[5]), false, false);
    const auto s1 = __builtin_amdgcn_permlane32_swap(pk(a[2], a[3]), pk(a[6], a[7]), false, false);
    char* op = K_OUT + (((bp * 2 + bi) * 64 + (gp >> 1) * 32 + r32) * 896 + br * 448 + m * 64 + ct * 32 + (gp & 1) * 16 + hh * 8) * 2;
    *(u32x4*)op = u32x4{s0[0], s1[0], s0[1], s1[1]};
    if (gp == 2 && m == 0 && w == 0 && lane == 0) __hip_atomic_store(epoch_p, want, __ATOMIC_RELAXED, __HIP_MEMORY_SCOPE_AGENT);
  }
  ST(14);
  STOUT();
}

extern "C" void kernel_launch(void* const* d_in, const int* in_sizes, int n_in, void* d_out, int out_size,
                              void* d_ws, size_t ws_size, hipStream_t stream) {
  Params p;
  p.x0 = (const char*)d_in[0]; p.k0 = (const char*)d_in[1]; p.v0 = (const char*)d_in[2]; p.wq0 = (const char*)d_in[3]; p.wo0 = (const char*)d_in[4];
  p.x1 = (const char*)d_in[5]; p.k1 = (const char*)d_in[6]; p.v1 = (const char*)d_in[7]; p.wq1 = (const char*)d_in[8]; p.wo1 = (const char*)d_in[9];
  p.out = (char*)d_out; p.ws = (char*)d_ws;
  gqa_fused<<<256, 512, 0, stream>>>(p);
}
```

```cpp
#include <hip/hip_runtime.h>
#include <hip/hip_bf16.h>

typedef short s16x4 __attribute__((ext_vector_type(4)));
typedef short bf16x8 __attribute__((ext_vector_type(8)));
typedef float f32x4 __attribute__((ext_vector_type(4)));
typedef float f32x2 __attribute__((ext_vector_type(2)));
typedef float f32x16 __attribute__((ext_vector_type(16)));
typedef unsigned u32x4 __attribute__((ext_vector_type(4)));
typedef unsigned u32x2 __attribute__((ext_vector_type(2)));
typedef unsigned u32x16 __attribute__((ext_vector_type(16)));
typedef unsigned u32x8 __attribute__((ext_vector_type(8)));
typedef __bf16 bf16x2_t __attribute__((ext_vector_type(2)));
#define LAS __attribute__((address_space(3)))
#define GAS __attribute__((address_space(1)))
#define CAS __attribute__((address_space(4)))
#define DI __device__ __forceinline__

#define ROT_B 2
#define ROT_H 2
#define POL ""
#define THR 10
#define P_EARLY 0
#define PFLAG_AT 0
#define ROT_Z 1

struct Params {
  const char* x0; const char* k0; const char* v0; const char* wq0; const char* wo0;
  const char* x1; const char* k1; const char* v1; const char* wq1; const char* wo1;
  char* out; char* ws;
};

constexpr unsigned WS_EPOCH = 0;
constexpr unsigned WS_PFLAG = 4096;
constexpr unsigned WS_FFLAG = 16384;
constexpr unsigned WS_P = 65536;
constexpr unsigned WS_F = WS_P + 32 * 65536;
constexpr unsigned WS_END = WS_F + 64 * 65536;

constexpr unsigned SW = 0;
constexpr unsigned SX = 131072;

#define ST(i) do {} while (0)
#define STDECL
#define STOUT() do {} while (0)

DI unsigned pk(float a, float b) { f32x2 v = {a, b}; return __builtin_bit_cast(unsigned, __builtin_convertvector(v, bf16x2_t)); }
DI bf16x8 pk8(f32x4 a, f32x4 b) { u32x4 r = {pk(a[0], a[1]), pk(a[2], a[3]), pk(b[0], b[1]), pk(b[2], b[3])}; return __builtin_bit_cast(bf16x8, r); }
DI bf16x8 ld128(unsigned a) { return *(LAS const bf16x8*)(size_t)a; }
DI bf16x8 ldtr(unsigned a0, unsigned a1) {
  s16x4 lo = __builtin_amdgcn_ds_read_tr16_b64_v4i16((LAS s16x4*)(size_t)a0);
  s16x4 hi = __builtin_amdgcn_ds_read_tr16_b64_v4i16((LAS s16x4*)(size_t)a1);
  return __builtin_shufflevector(lo, hi, 0, 1, 2, 3, 4, 5, 6, 7);
}
#define MFMA(a, b, c) __builtin_amdgcn_mfma_f32_16x16x32_bf16((a), (b), (c), 0, 0, 0)
#define MFMA32(a, b, c) __builtin_amdgcn_mfma_f32_32x32x16_bf16((a), (b), (c), 0, 0, 0)
#define ZERO4 (f32x4{0.f, 0.f, 0.f, 0.f})
#define ZERO16 (f32x16{0.f, 0.f, 0.f, 0.f, 0.f, 0.f, 0.f, 0.f, 0.f, 0.f, 0.f, 0.f, 0.f, 0.f, 0.f, 0.f})
DI bf16x8 pk16(const f32x16& x, int a) {
  u32x4 r = {pk(x[8 * a], x[8 * a + 1]), pk(x[8 * a + 2], x[8 * a + 3]), pk(x[8 * a + 4], x[8 * a + 5]), pk(x[8 * a + 6], x[8 * a + 7])};
  return __builtin_bit_cast(bf16x8, r);
}
#define BAR_C() do { __builtin_amdgcn_sched_barrier(0); asm volatile("" ::: "memory"); __builtin_amdgcn_s_waitcnt(0xC07F); \
                     __builtin_amdgcn_s_barrier(); asm volatile("" ::: "memory"); __builtin_amdgcn_sched_barrier(0); } while (0)
#define BAR_L(n) asm volatile("s_waitcnt vmcnt(" #n ")\n\ts_barrier" ::: "memory")
#define SG_MFMA(n) __builtin_amdgcn_sched_group_barrier(0x008, (n), 0)
#define SG_DSR(n) __builtin_amdgcn_sched_group_barrier(0x100, (n), 0)
#define SG_VALU(n) __builtin_amdgcn_sched_group_barrier(0x002, (n), 0)
#define STA(i)
#define STB(i) ST(i)
#define NOLOAD 0
DI void glds(const char* sbase, unsigned voff, unsigned lds_addr) {
  if (NOLOAD) return;
  asm volatile("s_mov_b32 m0, %2\n\ts_nop 0\n\tglobal_load_lds_dwordx4 %0, %1" POL "\n\ts_waitcnt vmcnt(%3)" :: "v"(voff), "s"(sbase), "s"(lds_addr), "i"(THR) : "memory");
}
DI void glds_dev(const char* sbase, unsigned voff, unsigned lds_addr) {
  asm volatile("s_mov_b32 m0, %2\n\ts_nop 0\n\tglobal_load_lds_dwordx4 %0, %1 sc1\n\ts_waitcnt vmcnt(%3)" :: "v"(voff), "s"(sbase), "s"(lds_addr), "i"(THR) : "memory");
}
DI unsigned next_epoch(unsigned e) { return e * 0x9E3779B1u + 0x7F4A7C15u; }
DI float xmax(float v) {
  auto a = __builtin_amdgcn_permlane32_swap(__float_as_uint(v), __float_as_uint(v), false, false);
  v = fmaxf(__uint_as_float(a[0]), __uint_as_float(a[1]));
  auto b = __builtin_amdgcn_permlane16_swap(__float_as_uint(v), __float_as_uint(v), false, false);
  return fmaxf(__uint_as_float(b[0]), __uint_as_float(b[1]));
}
DI float xsum(float v) {
  auto a = __builtin_amdgcn_permlane32_swap(__float_as_uint(v), __float_as_uint(v), false, false);
  v = __uint_as_float(a[0]) + __uint_as_float(a[1]);
  auto b = __builtin_amdgcn_permlane16_swap(__float_as_uint(v), __float_as_uint(v), false, false);
  return __uint_as_float(b[0]) + __uint_as_float(b[1]);
}
constexpr int zslot(int z) { return (0x10213021 >> (4 * z)) & 3; }
constexpr int CODE_BYTES = 21504;
constexpr unsigned SPIN_CAP = 1u << 18;

__global__ __launch_bounds__(512, 2) void gqa_fused(Params P) {
  __shared__ __attribute__((aligned(1024))) char lds[163840];
  u32x16 ka; u32x8 kb8;
  asm volatile("s_load_dwordx16 %0, %2, 0x0\n\ts_load_dwordx8 %1, %2, 0x40" : "=&s"(ka), "=&s"(kb8) : "s"(__builtin_amdgcn_kernarg_segment_ptr()));
  const int tid = threadIdx.x, lane = tid & 63;
  const int w = __builtin_amdgcn_readfirstlane(tid >> 6);
  const int bid = blockIdx.x;
  const int xcd = bid & 7, sl = bid >> 3;
  const int br = xcd >> 2, gp = xcd & 3;
  const int bp = sl >> 3, m = sl & 7;
  const int hj = m & 3, h = gp * 4 + hj, b = bp * 2 + (m >> 2);
  const int team = xcd * 4 + bp, comp = br * 4 + bp, grp = comp * 8 + m;
  const unsigned lds0 = (unsigned)(size_t)(LAS char*)lds;
  const int g1 = ROT_Z ? (bp + m) & 1 : 0, k1 = ROT_Z ? (((bp + m) >> 1) & 1) * 2 + (m >> 2) : 0;
  STDECL;
  ST(0);
  {
    unsigned long long pc; asm volatile("s_getpc_b64 %0" : "=s"(pc));
    const unsigned vo = min((sl * 8 + w) * 128, CODE_BYTES - 128);
    asm volatile("s_mov_b32 m0, %2\n\ts_nop 0\n\tglobal_load_lds_dword %0, %1" :: "v"(vo), "s"(pc & ~127ull), "s"(lds0 + SX + 3 * 8192 + w * 256) : "memory");
  }
  const int lw = w & 3;
  const unsigned hi = lane >> 5, l31 = lane & 31;
  unsigned v_x = (lane >> 3) * 896 + (((lane & 7) ^ (4 * (lw & 1) + (lane >> 4))) * 16);
  unsigned v_wq = hi * 8192 + ((l31 ^ ((2 * (lw & 1) + hi) << 2)) * 16);
  int sa = ((ROT_B == 0 ? 0 : ROT_B == 1 ? b : ROT_B == 2 ? (b >> 1) * 2 : ROT_B == 3 ? (b >> 2) * 4 : ROT_B == 4 ? (b & 3) * 2 : (b >> 1) * 3) + ROT_H * hj);
  sa = sa >= 7 ? sa - 7 : sa; sa = sa >= 7 ? sa - 7 : sa; if (ROT_B == 5 || ROT_H > 2) sa = sa >= 7 ? sa - 7 : sa;
  int ox = b * 57344 + lw * (8 * 896), owq = h * 512 + lw * (2 * 8192);
  asm volatile("" : "+v"(v_x), "+v"(v_wq));
  asm volatile("s_waitcnt lgkmcnt(0)" : "+s"(ka), "+s"(kb8), "+s"(sa), "+s"(ox), "+s"(owq));
#define KPTR(lo, hi) ((char*)(((unsigned long long)(hi) << 32) | (lo)))
#define K_X (br ? KPTR(ka[10], ka[11]) : KPTR(ka[0], ka[1]))
#define K_K (br ? KPTR(ka[12], ka[13]) : KPTR(ka[2], ka[3]))
#define K_V (br ? KPTR(ka[14], ka[15]) : KPTR(ka[4], ka[5]))
#define K_WQ (br ? KPTR(kb8[0], kb8[1]) : KPTR(ka[6], ka[7]))
#define K_WO (br ? KPTR(kb8[2], kb8[3]) : KPTR(ka[8], ka[9]))
#define K_OUT KPTR(kb8[4], kb8[5])
#define K_WS KPTR(kb8[6], kb8[7])

  if (__builtin_expect(w >= 4, 1)) {
    __builtin_amdgcn_s_setprio(3);
    const char* const xb = K_X + ox;
    const char* const wqb = K_WQ + owq;
    auto stageA = [&](int slot) {
      const char* wq = wqb + sa * (64 * 8192);
      const unsigned dw = lds0 + SW + slot * 32768 + lw * 1024;
      const char* xs = xb + sa * 128;
      const unsigned dx = lds0 + SX + slot * 8192 + lw * 1024;
      glds(xs, v_x, dx);
      glds(xs + 32 * 896, v_x, dx + 4096);
#pragma unroll
      for (int i = 0; i < 8; ++i) glds(wq + i * (8 * 8192), v_wq, dw + i * 4096);
      sa = sa == 6 ? 0 : sa + 1;
    };
    stageA(0);
    char* const ws = K_WS;
    GAS unsigned* const epoch_p = (GAS unsigned*)(ws + WS_EPOCH + comp * 256);
    GAS unsigned* const pflag_p = (GAS unsigned*)(ws + WS_PFLAG + team * 256);
    unsigned ep = *(const CAS unsigned*)(size_t)epoch_p;
    const unsigned v_k = hi * 512 + ((l31 ^ (2 * lw + hi)) * 16);
    const unsigned v_wo = (lane >> 3) * 896 + (((lane & 7) ^ (((lane >> 4) & 1) << 2)) * 16);
    const unsigned v_v = (lane >> 3) * 512 + (((lane & 7) ^ (4 * (lw & 1) + (lane >> 4))) * 16);
    const unsigned v_k1 = v_k ^ 128;
    const char* const kb = K_K + (b * 8 + (h >> 1)) * 32768 + lw * 1024;
    const char* const vb = K_V + (bp * 16 + gp * 2) * 32768 + lw * 4096;
    const char* const wob = K_WO + gp * (4 * 229376) + m * 128 + lw * (8 * 896);
    auto stageKV = [&](const char* src, int slot, int first) {
#pragma unroll
      for (int i = 0; i < 8; ++i) { const int ii = (i + 4 * first) & 7; glds(src + ii * 4096, (i & 1) ? v_k1 : v_k, lds0 + SW + slot * 32768 + lw * 1024 + ii * 4096); }
    };
    auto stageZ = [&](int z, int part) {
      const int gl = g1 ^ (z >> 2), kq = (k1 + z) & 3;
      const char* sw = wob + gl * (2 * 229376) + kq * (64 * 896);
      const char* sv = vb + gl * 32768 + kq * 128;
      const unsigned d = lds0 + SW + zslot(z) * 32768 + lw * 1024;
      if (part & 1) {
#pragma unroll
        for (int i = 0; i < 4; ++i) glds(sw + (i >> 1) * 229376 + (i & 1) * (32 * 896), v_wo, d + i * 4096);
      }
      if (part & 2) {
#pragma unroll
        for (int i = 0; i < 4; ++i) glds(sv + (i >> 1) * (8 * 32768) + (i & 1) * 16384, v_v, d + 16384 + i * 4096);
      }
    };
    stageA(1);
    ST(10);
    BAR_L(10); stageA(2);
    asm volatile("" : "+s"(ep));
    ST(1);
#pragma unroll 1
    for (int t = 0; t < 4; ++t) {
      BAR_L(10);
      stageA(t == 3 ? 0 : t);
    }
    BAR_L(10); STA(15); stageKV(kb, 3, h & 1);
    if (m == 7) {
      BAR_L(8); BAR_L(63); BAR_L(0);
      STOUT();
      return;
    }
    BAR_L(8); STA(6); stageZ(0, 3);
    ST(2);
    BAR_L(63);
    BAR_L(8); stageZ(1, 3); stageZ(2, 1);
    ST(3);
    BAR_L(12); stageZ(2, 2); stageZ(3, 1);
    ST(4);
    BAR_L(12); STB(11); stageZ(3, 2); stageZ(4, 1);
    BAR_L(12); STB(12); stageZ(4, 2); stageZ(5, 1);
    ST(5);
    unsigned fl;
    const unsigned voff_f = (lane & 31) * 4;
    const unsigned want = next_epoch(ep);
    auto stageP = [&](int k0, int k1_) {
      const char* src = ws + WS_P + team * 65536 + lw * 2048;
      const unsigned v_lin = lane * 16;
#pragma unroll 1
      for (int k = k0; k < k1_; ++k) {
        const int mm = ((m + k) & 3) | ((m ^ k) & 4);
        const unsigned dst = lds0 + ((k & 4) ? SW + 3 * 32768 : SX) + (mm & 3) * 8192 + lw * 2048;
        glds_dev(src + mm * 8192, v_lin, dst);
        glds_dev(src + mm * 8192 + 1024, v_lin, dst + 1024);
      }
    };
    BAR_L(12); STB(13); stageZ(5, 2);
    asm volatile("global_load_dword %0, %1, %2 sc1" : "=v"(fl) : "v"(voff_f), "s"(pflag_p) : "memory");
    stageZ(6, 1);
    BAR_L(13); STB(14); stageZ(6, 2);
    asm volatile("s_waitcnt vmcnt(8)" : "+v"(fl) :: "memory");
    for (unsigned spins = 0; !__all(fl == want) && spins < SPIN_CAP; ++spins)
      asm volatile("global_load_dword %0, %1, %2 sc1\n\ts_waitcnt vmcnt(0)" : "=v"(fl) : "v"(voff_f), "s"(pflag_p) : "memory");
    ST(7);
    stageP(1, 4);
    BAR_L(14); STB(15); stageP(4, 8);
    BAR_L(14); stageZ(7, 3);
    ST(8);
    BAR_L(8);
    BAR_L(0);
    ST(9);
    STOUT();
    return;
  }

  const unsigned r16 = lane & 15, g = lane >> 4, q4 = r16 >> 2, p4 = lane & 3;
  const unsigned r32 = lane & 31, hh = lane >> 5, blk = g & 1;
  asm volatile("s_waitcnt vmcnt(0)" ::: "memory");
  char* const ws = K_WS;
  GAS unsigned* const epoch_p = (GAS unsigned*)(ws + WS_EPOCH + comp * 256);
  GAS unsigned* const pflag_p = (GAS unsigned*)(ws + WS_PFLAG + team * 256);
  const unsigned ep = *(const CAS unsigned*)(size_t)epoch_p;
  const __amdgpu_buffer_rsrc_t rs = __builtin_amdgcn_make_buffer_rsrc(ws, 0, WS_END, 0x00020000);
  f32x16 acc[2][2];
  bf16x8 fa0[2][2], fb0[2][2], fa1[2][2], fb1[2][2];
  auto mm = [&](const bf16x8 (&fa)[2][2], const bf16x8 (&fb)[2][2], bool first) {
#pragma unroll
    for (int s = 0; s < 2; ++s)
#pragma unroll
      for (int i = 0; i < 2; ++i)
#pragma unroll
        for (int j = 0; j < 2; ++j) acc[i][j] = MFMA32(fa[s][i], fb[s][j], (first && s == 0) ? ZERO16 : acc[i][j]);
  };
#define SG_HALF() do { _Pragma("unroll") for (int i_ = 0; i_ < 4; ++i_) { SG_MFMA(1); SG_DSR(3); } SG_MFMA(4); } while (0)
#define PIN_ACC() asm volatile("" : "+v"(acc[0][0]), "+v"(acc[0][1]), "+v"(acc[1][0]), "+v"(acc[1][1]))
  {
    const unsigned xa = lds0 + SX + r32 * 128 + ((hh ^ ((r32 >> 1) & 7)) * 16);
    const unsigned wa = lds0 + SW + (8 * hh + q4) * 512 + (((8 * w + 2 * blk + (p4 >> 1)) ^ (q4 << 2)) * 16) + (p4 & 1) * 8;
    auto rd = [&](bf16x8 (&fa)[2][2], bf16x8 (&fb)[2][2], int slot, int half) {
#pragma unroll
      for (int s = 0; s < 2; ++s)
#pragma unroll
        for (int i = 0; i < 2; ++i) {
          const int ks = 2 * half + s;
          const unsigned wb = (wa ^ (i * 64)) + (slot >> 1) * 65536, wo_ = (slot & 1) * 32768 + ks * 8192;
          fa[s][i] = ldtr(wb + wo_, wb + wo_ + 2048);
          fb[s][i] = ld128((xa ^ (ks * 32)) + slot * 8192 + i * 4096);
        }
    };
    BAR_C();
    ST(1);
    rd(fa0, fb0, 0, 0);
#pragma unroll
    for (int t = 0; t < 7; ++t) {
      rd(fa1, fb1, t % 3, 1);
      mm(fa0, fb0, t == 0);
      SG_HALF();
      PIN_ACC();
      BAR_C();
      if (t < 6) rd(fa0, fb0, (t + 1) % 3, 0);
      mm(fa1, fb1, false);
      if (t < 6) { SG_HALF(); PIN_ACC(); __builtin_amdgcn_sched_barrier(0); }
    }
  }
  ST(2);
  {
    const unsigned qa = lds0 + SX + (r32 >> 4) * 8192 + w * 2048 + r16 * 16 + hh * 8;
#pragma unroll
    for (int i = 0; i < 2; ++i)
#pragma unroll
      for (int j = 0; j < 2; ++j)
#pragma unroll
        for (int a = 0; a < 4; ++a)
          *(LAS u32x2*)(size_t)(qa + j * 16384 + i * 1024 + a * 256) = u32x2{pk(acc[i][j][4 * a], acc[i][j][4 * a + 1]), pk(acc[i][j][4 * a + 2], acc[i][j][4 * a + 3])};
  }
  BAR_C();
  ST(3);
  const unsigned want = next_epoch(ep);
  u32x4 pv[2];
  unsigned plo;
  {
    f32x4 sc[4];
#pragma unroll
    for (int i = 0; i < 4; ++i) sc[i] = ZERO4;
    const unsigned own = lds0 + SX + w * 8192 + lane * 16;
    const unsigned ka = lds0 + SW + 3 * 32768 + r16 * 512 + ((g ^ r16) * 16);
#pragma unroll
    for (int s = 0; s < 8; ++s) {
      const bf16x8 qf = ld128(own + s * 1024);
#pragma unroll
      for (int kt = 0; kt < 4; ++kt) sc[kt] = MFMA(ld128((ka ^ ((s & 3) * 64)) + kt * 8192 + (s >> 2) * 256), qf, sc[kt]);
    }
    SG_VALU(4); SG_DSR(10);
#pragma unroll
    for (int i = 0; i < 30; ++i) { SG_MFMA(1); SG_DSR(1); }
    SG_MFMA(2);
    ST(4);
    float mx = fmaxf(fmaxf(fmaxf(sc[0][0], sc[0][1]), fmaxf(sc[0][2], sc[0][3])), fmaxf(fmaxf(sc[1][0], sc[1][1]), fmaxf(sc[1][2], sc[1][3])));
    mx = fmaxf(mx, fmaxf(fmaxf(fmaxf(sc[2][0], sc[2][1]), fmaxf(sc[2][2], sc[2][3])), fmaxf(fmaxf(sc[3][0], sc[3][1]), fmaxf(sc[3][2], sc[3][3]))));
    mx = xmax(mx);
    constexpr float C = 0.0625f * 1.4426950408889634f;
    const float mc = mx * C;
    float l = 0.f;
#pragma unroll
    for (int kt = 0; kt < 4; ++kt)
#pragma unroll
      for (int r = 0; r < 4; ++r) { sc[kt][r] = __builtin_amdgcn_exp2f(fmaf(sc[kt][r], C, -mc)); l += sc[kt][r]; }
    l = xsum(l);
    const float inv = __builtin_amdgcn_rcpf(l);
    const unsigned po = (w >> 1) * 4096 + hh * 1024 + ((w & 1) * 16 + r16 + 32 * blk) * 16;
    const unsigned go = WS_P + team * 65536 + m * 8192 + po;
    plo = lds0 + SX + (m & 3) * 8192 + po;
#pragma unroll
    for (int e = 0; e < 2; ++e) {
      const f32x4 a = sc[2 * e] * inv, c = sc[2 * e + 1] * inv;
      const auto s0 = __builtin_amdgcn_permlane32_swap(pk(a[0], a[1]), pk(c[0], c[1]), false, false);
      const auto s1 = __builtin_amdgcn_permlane32_swap(pk(a[2], a[3]), pk(c[2], c[3]), false, false);
      const u32x4 v = {s0[0], s1[0], s0[1], s1[1]};
      __builtin_amdgcn_raw_buffer_store_b128(v, rs, go + e * 2048, 0, 16);
      pv[e] = v;
    }
  }
  ST(5);
  if (m == 7) {
    asm volatile("s_waitcnt vmcnt(0)" ::: "memory");
    if (lane == 0) __hip_atomic_store(pflag_p + m * 4 + w, want, __ATOMIC_RELAXED, __HIP_MEMORY_SCOPE_AGENT);
    STOUT();
    return;
  }
  const int ct = w & 1, bi = w >> 1;
  f32x16 o[2];
  {
    const unsigned wa = lds0 + SW + (8 * hh + q4) * 128 + (((4 * ct + 2 * blk + (p4 >> 1)) ^ ((q4 >> 1) << 2)) * 16) + (p4 & 1) * 8;
    const unsigned va = lds0 + SW + 16384 + bi * 8192 + r32 * 128 + ((hh ^ ((r32 >> 1) & 7)) * 16);
    auto rd = [&](bf16x8 (&fa)[2][2], bf16x8 (&fb)[2][2], int slot, int half) {
#pragma unroll
      for (int s = 0; s < 2; ++s)
#pragma unroll
        for (int i = 0; i < 2; ++i) {
          const int ks = 2 * half + s;
          const unsigned hi = (slot >> 1) * 65536, lo = (slot & 1) * 32768;
          fa[s][i] = ld128(((va ^ (ks * 32)) + hi) + lo + i * 4096);
          fb[s][i] = ldtr((wa + hi) + lo + i * 8192 + ks * 2048, (wa + hi) + lo + i * 8192 + ks * 2048 + 512);
        }
    };
    auto packC = [&](bf16x8 (&cf)[2][4]) {
#pragma unroll
      for (int e = 0; e < 2; ++e)
#pragma unroll
        for (int s = 0; s < 4; ++s) cf[e][s] = pk16(acc[s >> 1][e], s & 1);
    };
    auto compPC = [&](const bf16x8 (&cf)[2][4], int gl, bool first) {
      const unsigned pa = lds0 + (bi != (m >> 2) ? SW + 3 * 32768 : SX) + gl * 16384 + lane * 16;
#pragma unroll
      for (int e = 0; e < 2; ++e)
#pragma unroll
        for (int s = 0; s < 4; ++s)
#pragma unroll
          for (int qt = 0; qt < 2; ++qt) o[qt] = MFMA32(cf[e][s], ld128(pa + e * 8192 + qt * 4096 + s * 1024), (first && e == 0 && s == 0) ? ZERO16 : o[qt]);
    };
    bf16x8 cf0[2][4], cf1[2][4];
    BAR_C();
    ST(6);
    rd(fa0, fb0, zslot(0), 0);
    *(LAS u32x4*)(size_t)plo = pv[0];
    *(LAS u32x4*)(size_t)(plo + 2048) = pv[1];
#pragma unroll
    for (int z = 0; z < 8; ++z) {
      rd(fa1, fb1, zslot(z), 1);
      mm(fa0, fb0, (z & 3) == 0);
      SG_HALF();
      PIN_ACC();
      if (z == PFLAG_AT) {
        asm volatile("s_waitcnt vmcnt(0)" ::: "memory");
        if (lane == 0) __hip_atomic_store(pflag_p + m * 4 + w, want, __ATOMIC_RELAXED, __HIP_MEMORY_SCOPE_AGENT);
      }
      if (z == 6) {
        BAR_C();
        compPC(cf0, g1, true);
        SG_DSR(4);
#pragma unroll
        for (int i = 0; i < 12; ++i) { SG_MFMA(1); SG_DSR(1); }
        SG_MFMA(4);
        asm volatile("" : "+v"(o[0]), "+v"(o[1]));
      }
      if (z < 7) {
        BAR_C();
        if (z == 4) ST(8);
        if (z == 5) ST(9);
        if (z == 6) ST(10);
        rd(fa0, fb0, zslot(z + 1), 0);
      }
      mm(fa1, fb1, false);
      if (z < 7) SG_HALF();
      if (z == 3) { packC(cf0); asm volatile("" : "+v"(cf0[0][0]), "+v"(cf0[0][1]), "+v"(cf0[0][2]), "+v"(cf0[0][3]), "+v"(cf0[1][0]), "+v"(cf0[1][1]), "+v"(cf0[1][2]), "+v"(cf0[1][3])); }
      else if (z < 7) PIN_ACC();
      __builtin_amdgcn_sched_barrier(0);
    }
    packC(cf1);
    compPC(cf1, g1 ^ 1, false);
  }
  ST(11);
  const unsigned fo = WS_F + grp * 65536 + gp * 4096 + w * 1024 + lane * 16;
  GAS unsigned* const fflag_p = (GAS unsigned*)(ws + WS_FFLAG + grp * 256);
#pragma unroll
  for (int k = 0; k < 4; ++k)
    if (gp != k) __builtin_amdgcn_raw_buffer_store_b128(__builtin_bit_cast(u32x4, pk16(o[k >> 1], k & 1)), rs, fo + k * 16384, 0, 16);
  asm volatile("s_waitcnt vmcnt(0)" ::: "memory");
  if (lane == 0) __hip_atomic_store(fflag_p + gp * 4 + w, want, __ATOMIC_RELAXED, __HIP_MEMORY_SCOPE_AGENT);
  ST(12);
  {
    float a[8];
#pragma unroll
    for (int i = 0; i < 8; ++i) a[i] = gp == 0 ? o[0][i] : gp == 1 ? o[0][8 + i] : gp == 2 ? o[1][i] : o[1][8 + i];
    GAS unsigned* const fp = fflag_p + (lane & 3) * 4 + w;
    for (unsigned spins = 0; spins < SPIN_CAP; ++spins) {
      const unsigned fl = __hip_atomic_load(fp, __ATOMIC_RELAXED, __HIP_MEMORY_SCOPE_AGENT);
      if (__all(fl == want || (lane & 3) == gp)) break;
    }
    asm volatile("" ::: "memory");
    ST(13);
    const unsigned fi = WS_F + grp * 65536 + gp * 16384 + w * 1024 + lane * 16;
    u32x4 d[3];
#pragma unroll
    for (int k = 0; k < 3; ++k) d[k] = __builtin_amdgcn_raw_buffer_load_b128(rs, fi + ((gp + 1 + k) & 3) * 4096, 0, 16);
#pragma unroll
    for (int k = 0; k < 3; ++k)
#pragma unroll
      for (int i = 0; i < 4; ++i) { a[2 * i] += __uint_as_float(d[k][i] << 16); a[2 * i + 1] += __uint_as_float(d[k][i] & 0xffff0000u); }
    const auto s0 = __builtin_amdgcn_permlane32_swap(pk(a[0], a[1]), pk(a[4], a[5]), false, false);
    const auto s1 = __builtin_amdgcn_permlane32_swap(pk(a[2], a[3]), pk(a[6], a[7]), false, false);
    char* op = K_OUT + (((bp * 2 + bi) * 64 + (gp >> 1) * 32 + r32) * 896 + br * 448 + m * 64 + ct * 32 + (gp & 1) * 16 + hh * 8) * 2;
    *(u32x4*)op = u32x4{s0[0], s1[0], s0[1], s1[1]};
    if (gp == 2 && m == 0 && w == 0 && lane == 0) __hip_atomic_store(epoch_p, want, __ATOMIC_RELAXED, __HIP_MEMORY_SCOPE_AGENT);
  }
  ST(14);
  STOUT();
}

extern "C" void kernel_launch(void* const* d_in, const int* in_sizes, int n_in, void* d_out, int out_size,
                              void* d_ws, size_t ws_size, hipStream_t stream) {
  Params p;
  p.x0 = (const char*)d_in[0]; p.k0 = (const char*)d_in[1]; p.v0 = (const char*)d_in[2]; p.wq0 = (const char*)d_in[3]; p.wo0 = (const char*)d_in[4];
  p.x1 = (const char*)d_in[5]; p.k1 = (const char*)d_in[6]; p.v1 = (const char*)d_in[7]; p.wq1 = (const char*)d_in[8]; p.wo1 = (const char*)d_in[9];
  p.out = (char*)d_out; p.ws = (char*)d_ws;
  gqa_fused<<<256, 512, 0, stream>>>(p);
}
```

```cpp
#include <hip/hip_runtime.h>
#include <hip/hip_bf16.h>

typedef short s16x4 __attribute__((ext_vector_type(4)));
typedef short bf16x8 __attribute__((ext_vector_type(8)));
typedef float f32x4 __attribute__((ext_vector_type(4)));
typedef float f32x2 __attribute__((ext_vector_type(2)));
typedef float f32x16 __attribute__((ext_vector_type(16)));
typedef unsigned u32x4 __attribute__((ext_vector_type(4)));
typedef unsigned u32x2 __attribute__((ext_vector_type(2)));
typedef unsigned u32x16 __attribute__((ext_vector_type(16)));
typedef unsigned u32x8 __attribute__((ext_vector_type(8)));
typedef __bf16 bf16x2_t __attribute__((ext_vector_type(2)));
#define LAS __attribute__((address_space(3)))
#define GAS __attribute__((address_space(1)))
#define CAS __attribute__((address_space(4)))
#define DI __device__ __forceinline__

#define ROT_B 2
#define ROT_H 2
#define POL ""
#define THR 9
#define P_EARLY 0
#define POLL_SLEEP 0
#define A16 1
#define B10_PROMPT 0
#define PFLAG_AT 1
#define ROT_Z 1

struct Params {
  const char* x0; const char* k0; const char* v0; const char* wq0; const char* wo0;
  const char* x1; const char* k1; const char* v1; const char* wq1; const char* wo1;
  char* out; char* ws;
};

constexpr unsigned WS_EPOCH = 0;
constexpr unsigned WS_PFLAG = 4096;
constexpr unsigned WS_FFLAG = 65536;
constexpr unsigned WS_P = 131072;
constexpr unsigned WS_F = WS_P + 32 * 65536;
constexpr unsigned WS_END = WS_F + 64 * 65536;

constexpr unsigned SW = 0;
constexpr unsigned SX = 131072;

#define ST(i) do {} while (0)
#define STDECL
#define STOUT() do {} while (0)

DI unsigned pk(float a, float b) { f32x2 v = {a, b}; return __builtin_bit_cast(unsigned, __builtin_convertvector(v, bf16x2_t)); }
DI bf16x8 pk8(f32x4 a, f32x4 b) { u32x4 r = {pk(a[0], a[1]), pk(a[2], a[3]), pk(b[0], b[1]), pk(b[2], b[3])}; return __builtin_bit_cast(bf16x8, r); }
DI bf16x8 ld128(unsigned a) { return *(LAS const bf16x8*)(size_t)a; }
DI bf16x8 ldtr(unsigned a0, unsigned a1) {
  s16x4 lo = __builtin_amdgcn_ds_read_tr16_b64_v4i16((LAS s16x4*)(size_t)a0);
  s16x4 hi = __builtin_amdgcn_ds_read_tr16_b64_v4i16((LAS s16x4*)(size_t)a1);
  return __builtin_shufflevector(lo, hi, 0, 1, 2, 3, 4, 5, 6, 7);
}
#define MFMA(a, b, c) __builtin_amdgcn_mfma_f32_16x16x32_bf16((a), (b), (c), 0, 0, 0)
#define MFMA32(a, b, c) __builtin_amdgcn_mfma_f32_32x32x16_bf16((a), (b), (c), 0, 0, 0)
#define ZERO4 (f32x4{0.f, 0.f, 0.f, 0.f})
#define ZERO16 (f32x16{0.f, 0.f, 0.f, 0.f, 0.f, 0.f, 0.f, 0.f, 0.f, 0.f, 0.f, 0.f, 0.f, 0.f, 0.f, 0.f})
DI bf16x8 pk16(const f32x16& x, int a) {
  u32x4 r = {pk(x[8 * a], x[8 * a + 1]), pk(x[8 * a + 2], x[8 * a + 3]), pk(x[8 * a + 4], x[8 * a + 5]), pk(x[8 * a + 6], x[8 * a + 7])};
  return __builtin_bit_cast(bf16x8, r);
}
#define BAR_C() do { __builtin_amdgcn_sched_barrier(0); asm volatile("" ::: "memory"); __builtin_amdgcn_s_waitcnt(0xC07F); \
                     __builtin_amdgcn_s_barrier(); asm volatile("" ::: "memory"); __builtin_amdgcn_sched_barrier(0); } while (0)
#define BAR_L(n) asm volatile("s_waitcnt vmcnt(" #n ")\n\ts_barrier" ::: "memory")
#define SG_MFMA(n) __builtin_amdgcn_sched_group_barrier(0x008, (n), 0)
#define SG_DSR(n) __builtin_amdgcn_sched_group_barrier(0x100, (n), 0)
#define SG_DSW(n) __builtin_amdgcn_sched_group_barrier(0x200, (n), 0)
#define SG_VALU(n) __builtin_amdgcn_sched_group_barrier(0x002, (n), 0)
#define STA(i)
#define STB(i) ST(i)
#define NOLOAD 0
#define GL(n, pol) "s_waitcnt vmcnt(%[thr])\n\tglobal_load_lds_dwordx4 %" #n ", %[sb]" pol "\n\ts_add_u32 m0, m0, 0x1000\n\t"
DI void glds2(const char* sb, unsigned lds_addr, unsigned v0, unsigned v1) {
  if (NOLOAD) return;
  asm volatile("s_mov_b32 m0, %[la]\n\t" GL(0, POL) GL(1, POL) :: "v"(v0), "v"(v1), [sb] "s"(sb), [la] "s"(lds_addr), [thr] "i"(THR) : "memory", "scc");
}
DI void glds4(const char* sb, unsigned lds_addr, const unsigned (&v)[4]) {
  if (NOLOAD) return;
  asm volatile("s_mov_b32 m0, %[la]\n\t" GL(0, POL) GL(1, POL) GL(2, POL) GL(3, POL)
               :: "v"(v[0]), "v"(v[1]), "v"(v[2]), "v"(v[3]), [sb] "s"(sb), [la] "s"(lds_addr), [thr] "i"(THR) : "memory", "scc");
}
DI void glds8(const char* sb, unsigned lds_addr, const unsigned (&v)[8]) {
  if (NOLOAD) return;
  asm volatile("s_mov_b32 m0, %[la]\n\t" GL(0, POL) GL(1, POL) GL(2, POL) GL(3, POL) GL(4, POL) GL(5, POL) GL(6, POL) GL(7, POL)
               :: "v"(v[0]), "v"(v[1]), "v"(v[2]), "v"(v[3]), "v"(v[4]), "v"(v[5]), "v"(v[6]), "v"(v[7]),
                  [sb] "s"(sb), [la] "s"(lds_addr), [thr] "i"(THR) : "memory", "scc");
}
DI void glds2_dev(const char* sb, unsigned lds_addr, unsigned v0, unsigned v1) {
  asm volatile("s_mov_b32 m0, %[la]\n\t" GL(0, " sc1") GL(1, " sc1") :: "v"(v0), "v"(v1), [sb] "s"(sb), [la] "s"(lds_addr), [thr] "i"(THR) : "memory", "scc");
}
DI unsigned next_epoch(unsigned e) { return e * 0x9E3779B1u + 0x7F4A7C15u; }
DI float max2(float a, float b) { return __builtin_elementwise_maximum(a, b); }
DI float max3(float a, float b, float c) { return max2(max2(a, b), c); }
DI float xmax(float v) {
  auto a = __builtin_amdgcn_permlane32_swap(__float_as_uint(v), __float_as_uint(v), false, false);
  v = max2(__uint_as_float(a[0]), __uint_as_float(a[1]));
  auto b = __builtin_amdgcn_permlane16_swap(__float_as_uint(v), __float_as_uint(v), false, false);
  return max2(__uint_as_float(b[0]), __uint_as_float(b[1]));
}
DI float xsum(float v) {
  auto a = __builtin_amdgcn_permlane32_swap(__float_as_uint(v), __float_as_uint(v), false, false);
  v = __uint_as_float(a[0]) + __uint_as_float(a[1]);
  auto b = __builtin_amdgcn_permlane16_swap(__float_as_uint(v), __float_as_uint(v), false, false);
  return __uint_as_float(b[0]) + __uint_as_float(b[1]);
}
constexpr int zslot(int z) { return (0x30213021 >> (4 * z)) & 3; }
constexpr int CODE_BYTES = 21376;
constexpr unsigned SPIN_CAP = 1u << 18;

__global__ __launch_bounds__(512, 2) void gqa_fused(Params P) {
  __shared__ __attribute__((aligned(1024))) char lds[163840];
  u32x16 ka; u32x8 kb8;
  asm volatile("s_load_dwordx16 %0, %2, 0x0\n\ts_load_dwordx8 %1, %2, 0x40" : "=&s"(ka), "=&s"(kb8) : "s"(__builtin_amdgcn_kernarg_segment_ptr()));
  const int tid = threadIdx.x, lane = tid & 63;
  const int w = __builtin_amdgcn_readfirstlane(tid >> 6);
  const int bid = blockIdx.x;
  const int xcd = bid & 7, sl = bid >> 3;
  const int br = xcd >> 2, gp = xcd & 3;
  const int bp = sl >> 3, m = sl & 7;
  const int hj = m & 3, h = gp * 4 + hj, b = bp * 2 + (m >> 2);
  const int team = xcd * 4 + bp, comp = br * 4 + bp, grp = comp * 8 + m;
  const unsigned lds0 = (unsigned)(size_t)(LAS char*)lds;
  const int g1 = ROT_Z ? (bp + m) & 1 : 0;
  const int k1 = ROT_Z == 0 ? 0 : ROT_Z == 1 ? (((bp + m) >> 1) & 1) * 2 + (m >> 2) : ROT_Z == 2 ? ((m >> 1) & 1) * 2 + (m >> 2)
               : ROT_Z == 3 ? (((bp + m) >> 1) & 1) * 2 : ((m >> 1) & 1) * 2;
  STDECL;
  ST(0);
  {
    unsigned long long pc; asm volatile("s_getpc_b64 %0" : "=s"(pc));
    const unsigned vo = min((sl * 8 + w) * 128, CODE_BYTES - 128);
    asm volatile("s_mov_b32 m0, %2\n\ts_nop 0\n\tglobal_load_lds_dword %0, %1" :: "v"(vo), "s"(pc & ~127ull), "s"(lds0 + SX + 3 * 8192 + w * 256) : "memory");
  }
  const int lw = w & 3;
  const unsigned hi = lane >> 5, l31 = lane & 31;
  unsigned v_x = (lane >> 3) * 896 + (((lane & 7) ^ (4 * (lw & 1) + (lane >> 4))) * 16);
  unsigned v_x1 = v_x + 32 * 896;
  unsigned v_wq[8];
#pragma unroll
  for (int i = 0; i < 8; ++i)
    v_wq[i] = hi * 8192 + i * (8 * 8192) + (l31 ^ (((2 * (lw & 1) + hi) | ((i & 1) << 2)) << 1)) * 16;
  int sa = ((ROT_B == 0 ? 0 : ROT_B == 1 ? b : ROT_B == 2 ? (b >> 1) * 2 : ROT_B == 3 ? (b >> 2) * 4 : ROT_B == 4 ? (b & 3) * 2 : (b >> 1) * 3) + ROT_H * hj);
  sa = sa >= 7 ? sa - 7 : sa; sa = sa >= 7 ? sa - 7 : sa; if (ROT_B == 5 || ROT_H > 2) sa = sa >= 7 ? sa - 7 : sa;
  int ox = b * 57344 + lw * (8 * 896), owq = h * 512 + lw * (2 * 8192);
  asm volatile("" : "+v"(v_x), "+v"(v_x1), "+v"(v_wq[0]), "+v"(v_wq[1]), "+v"(v_wq[2]), "+v"(v_wq[3]), "+v"(v_wq[4]), "+v"(v_wq[5]), "+v"(v_wq[6]), "+v"(v_wq[7]));
  asm volatile("s_waitcnt lgkmcnt(0)" : "+s"(ka), "+s"(kb8), "+s"(sa), "+s"(ox), "+s"(owq));
#define KPTR(lo, hi) ((char*)(((unsigned long long)(hi) << 32) | (lo)))
#define K_X (br ? KPTR(ka[10], ka[11]) : KPTR(ka[0], ka[1]))
#define K_K (br ? KPTR(ka[12], ka[13]) : KPTR(ka[2], ka[3]))
#define K_V (br ? KPTR(ka[14], ka[15]) : KPTR(ka[4], ka[5]))
#define K_WQ (br ? KPTR(kb8[0], kb8[1]) : KPTR(ka[6], ka[7]))
#define K_WO (br ? KPTR(kb8[2], kb8[3]) : KPTR(ka[8], ka[9]))
#define K_OUT KPTR(kb8[4], kb8[5])
#define K_WS KPTR(kb8[6], kb8[7])

  if (__builtin_expect(w >= 4, 1)) {
    __builtin_amdgcn_s_setprio(3);
    const char* const xb = K_X + ox;
    const char* const wqb = K_WQ + owq;
    auto stageA = [&](int slot) {
      glds2(xb + sa * 128, lds0 + SX + slot * 8192 + lw * 1024, v_x, v_x1);
      glds8(wqb + sa * (64 * 8192), lds0 + SW + slot * 32768 + lw * 1024, v_wq);
      sa = sa == 6 ? 0 : sa + 1;
    };
    stageA(0);
    char* const ws = K_WS;
    GAS unsigned* const epoch_p = (GAS unsigned*)(ws + WS_EPOCH + comp * 256);
    GAS unsigned* const pflag_p = (GAS unsigned*)(ws + WS_PFLAG + team * 256);
    unsigned ep = *(const CAS unsigned*)(size_t)epoch_p;
    unsigned v_k[4], v_wo[4], v_v[4];
#pragma unroll
    for (int i = 0; i < 4; ++i) {
      v_k[i] = ((hi * 512 + ((l31 ^ (2 * lw + hi)) * 16)) ^ ((i & 1) * 128)) + i * 4096;
      v_wo[i] = (lane >> 3) * 896 + (((lane & 7) ^ ((((lane >> 4) & 1) | ((lw & 1) << 1)) << 1)) * 16) + (i >> 1) * 229376 + (i & 1) * (32 * 896);
      v_v[i] = (lane >> 3) * 512 + (((lane & 7) ^ (4 * (lw & 1) + (lane >> 4))) * 16) + (i >> 1) * (8 * 32768) + (i & 1) * 16384;
    }
    const char* const kb = K_K + (b * 8 + (h >> 1)) * 32768 + lw * 1024;
    const char* const vb = K_V + (bp * 16 + gp * 2) * 32768 + lw * 4096;
    const char* const wob = K_WO + gp * (4 * 229376) + m * 128 + lw * (8 * 896);
    auto stageK = [&]() {
      const int f = (h & 1) * 16384;
      glds4(kb + f, lds0 + SW + 3 * 32768 + lw * 1024 + f, v_k);
      glds4(kb + (f ^ 16384), lds0 + SW + 3 * 32768 + lw * 1024 + (f ^ 16384), v_k);
    };
    auto stageZ = [&](int z, int part) {
      const int gl = g1 ^ (z >> 2), kq = (k1 + z) & 3;
      const unsigned d = lds0 + SW + zslot(z) * 32768 + lw * 1024;
      if (part & 1) glds4(wob + gl * (2 * 229376) + kq * (64 * 896), d, v_wo);
      if (part & 2) glds4(vb + gl * 32768 + kq * 128, d + 16384, v_v);
    };
    stageA(1);
    ST(10);
    BAR_L(10); stageA(2);
    asm volatile("" : "+s"(ep));
    ST(1);
#pragma unroll 1
    for (int t = 0; t < 4; ++t) {
      BAR_L(10);
      stageA(t == 3 ? 0 : t);
    }
    BAR_L(10); STA(15); stageK();
    if (m == 7) {
      BAR_L(8); BAR_L(63); BAR_L(0);
      STOUT();
      return;
    }
    BAR_L(8); STA(6); stageZ(0, 3);
    ST(2);
    BAR_L(63);
    BAR_L(8); stageZ(1, 3); stageZ(2, 1);
    ST(3);
    BAR_L(12); stageZ(2, 2); stageZ(3, 1);
    ST(4);
    BAR_L(12); STB(11); stageZ(3, 2); stageZ(4, 1);
    BAR_L(12); STB(12); stageZ(4, 2); stageZ(5, 1);
    ST(5);
    unsigned fl;
    const unsigned voff_f = (lane & 31) * 4;
    const unsigned want = next_epoch(ep);
    auto stageP = [&](int k0, int k1_) {
      const char* src = ws + WS_P + team * 65536 + lw * 1024;
      const unsigned v_lin = lane * 16, v_lin1 = v_lin + 4096;
#pragma unroll
      for (int k = k0; k < k1_; ++k) {
        const int mm = ((m + k) & 3) | ((m ^ k) & 4);
        glds2_dev(src + mm * 8192, lds0 + ((k & 4) ? SW + 1 * 32768 : SX) + (mm & 3) * 8192 + lw * 1024, v_lin, v_lin1);
      }
    };
    BAR_L(12); STB(13); stageZ(5, 2); stageZ(6, 1);
    BAR_L(12); STB(14);
    asm volatile("global_load_dword %0, %1, %2 sc1" : "=v"(fl) : "v"(voff_f), "s"(pflag_p) : "memory");
    stageZ(6, 2); stageZ(7, 1);
    BAR_L(13); STB(15); stageZ(7, 2);
    asm volatile("s_waitcnt vmcnt(12)" : "+v"(fl) :: "memory");
    for (unsigned spins = 0; !__all(fl == want) && spins < SPIN_CAP; ++spins)
      asm volatile("s_sleep 1\n\tglobal_load_dword %0, %1, %2 sc1\n\ts_waitcnt vmcnt(0)" : "=v"(fl) : "v"(voff_f), "s"(pflag_p) : "memory");
    ST(7);
    stageP(1, 3);
    BAR_L(12); stageP(3, 7);
    ST(8);
    BAR_L(12); stageP(7, 8);
    BAR_L(0);
    ST(9);
    STOUT();
    return;
  }

  const unsigned r16 = lane & 15, g = lane >> 4, q4 = r16 >> 2, p4 = lane & 3;
  const unsigned r32 = lane & 31, hh = lane >> 5, blk = g & 1;
  asm volatile("s_waitcnt vmcnt(0)" ::: "memory");
  char* const ws = K_WS;
  GAS unsigned* const epoch_p = (GAS unsigned*)(ws + WS_EPOCH + comp * 256);
  GAS unsigned* const pflag_p = (GAS unsigned*)(ws + WS_PFLAG + team * 256);
  const unsigned ep = *(const CAS unsigned*)(size_t)epoch_p;
  const __amdgpu_buffer_rsrc_t rs = __builtin_amdgcn_make_buffer_rsrc(ws, 0, WS_END, 0x00020000);
  const int ct = w & 1, bi = w >> 1;
  unsigned qa = lds0 + SX + w * 2048 + (g >> 1) * 256 + r16 * 16 + (g & 1) * 8;
  unsigned own = lds0 + SX + w * 8192 + lane * 16;
  unsigned kad[4];
#pragma unroll
  for (int i = 0; i < 4; ++i) kad[i] = (lds0 + SW + 3 * 32768 + r16 * 512 + ((g ^ r16) * 16)) ^ (i * 64);
  const unsigned po = w * 2048 + lane * 16;
  unsigned pgo = WS_P + team * 65536 + m * 8192 + po;
  unsigned plo = lds0 + SX + (m & 3) * 8192 + po;
  unsigned wba[2][2], vba[2][2], pad[4];
#pragma unroll
  for (int u = 0; u < 2; ++u)
#pragma unroll
    for (int i = 0; i < 2; ++i) {
      wba[u][i] = lds0 + SW + u * 65536 + (8 * g + q4) * 128 + (((4 * ct + 2 * i + (p4 >> 1)) ^ (((q4 >> 1) | ((g & 1) << 1)) << 1)) * 16) + (p4 & 1) * 8;
      vba[u][i] = lds0 + SW + u * 65536 + 16384 + bi * 8192 + r16 * 128 + (((4 * i + g) ^ ((r16 >> 1) & 7)) * 16);
    }
#pragma unroll
  for (int i = 0; i < 4; ++i) pad[i] = lds0 + (bi != (m >> 2) ? SW + 1 * 32768 : SX) + g1 * 16384 + (gp ^ i) * 2048 + lane * 16;
  asm volatile("" : "+v"(wba[0][0]), "+v"(wba[0][1]), "+v"(wba[1][0]), "+v"(wba[1][1]), "+v"(vba[0][0]), "+v"(vba[0][1]), "+v"(vba[1][0]), "+v"(vba[1][1]),
                    "+v"(pad[0]), "+v"(pad[1]), "+v"(pad[2]), "+v"(pad[3]));
  unsigned fo = WS_F + grp * 65536 + gp * 4096 + w * 1024 + lane * 16;
  unsigned fi = WS_F + grp * 65536 + gp * 16384 + w * 1024 + lane * 16;
  GAS unsigned* const fflag_p = (GAS unsigned*)(ws + WS_FFLAG + grp * 1024 + w * 64);
  GAS unsigned* fp = fflag_p + gp * 64 + (lane & 3);
  GAS unsigned* fs = fflag_p + (lane & 3) * 64 + gp;
  char* op = K_OUT + (((bp * 2 + bi) * 64 + gp * 16 + r16) * 896 + br * 448 + m * 64 + ct * 32 + (g & 1) * 16 + (g >> 1) * 8) * 2;
  asm volatile("" : "+v"(fo), "+v"(fi), "+v"(op), "+v"(fp), "+v"(fs));
  asm volatile("" : "+v"(qa), "+v"(own), "+v"(kad[0]), "+v"(kad[1]), "+v"(kad[2]), "+v"(kad[3]), "+v"(pgo), "+v"(plo));
  {
    f32x4 qc[4][4];
    bf16x8 ga0[4], gb0[4], ga1[4], gb1[4];
    const unsigned xa = lds0 + SX + r16 * 128 + ((g ^ ((r16 >> 1) & 7)) * 16);
    const unsigned wa = lds0 + SW + (8 * g + q4) * 512 + (((8 * w + (p4 >> 1)) ^ ((q4 | ((g & 1) << 2)) << 1)) * 16) + (p4 & 1) * 8;
    unsigned xad[2] = {xa, xa ^ 64}, wad[2][4];
#pragma unroll
    for (int i = 0; i < 4; ++i) { wad[0][i] = wa ^ (i * 32); wad[1][i] = wad[0][i] + 65536; }
    asm volatile("" : "+v"(xad[0]), "+v"(xad[1]), "+v"(wad[0][0]), "+v"(wad[0][1]), "+v"(wad[0][2]), "+v"(wad[0][3]),
                      "+v"(wad[1][0]), "+v"(wad[1][1]), "+v"(wad[1][2]), "+v"(wad[1][3]));
    auto rd = [&](bf16x8 (&fa)[4], bf16x8 (&fb)[4], int slot, int half) {
#pragma unroll
      for (int i = 0; i < 4; ++i) {
        const unsigned wo_ = (slot & 1) * 32768 + half * 16384;
        fb[i] = ld128(xad[half] + slot * 8192 + i * 2048);
        fa[i] = ldtr(wad[slot >> 1][i] + wo_, wad[slot >> 1][i] + wo_ + 2048);
      }
    };
    auto mm16 = [&](const bf16x8 (&fa)[4], const bf16x8 (&fb)[4], bool first) {
#pragma unroll
      for (int i = 0; i < 4; ++i)
#pragma unroll
        for (int j = 0; j < 4; ++j) qc[i][j] = MFMA(fa[i], fb[j], first ? ZERO4 : qc[i][j]);
    };
#define SG_HALF16() do { SG_MFMA(1); SG_DSR(2); SG_MFMA(1); SG_DSR(2); _Pragma("unroll") for (int i_ = 0; i_ < 8; ++i_) { SG_MFMA(1); SG_DSR(1); } SG_MFMA(6); } while (0)
#define PIN_QC() asm volatile("" : "+v"(qc[0][0]), "+v"(qc[0][1]), "+v"(qc[0][2]), "+v"(qc[0][3]), "+v"(qc[1][0]), "+v"(qc[1][1]), "+v"(qc[1][2]), "+v"(qc[1][3]), \
                                   "+v"(qc[2][0]), "+v"(qc[2][1]), "+v"(qc[2][2]), "+v"(qc[2][3]), "+v"(qc[3][0]), "+v"(qc[3][1]), "+v"(qc[3][2]), "+v"(qc[3][3]))
    BAR_C();
    ST(1);
    rd(ga0, gb0, 0, 0);
    __builtin_amdgcn_sched_barrier(0);
#pragma unroll
    for (int t = 0; t < 7; ++t) {
      rd(ga1, gb1, t % 3, 1);
      mm16(ga0, gb0, t == 0);
      SG_HALF16();
      PIN_QC();
      BAR_C();
      if (t < 6) {
        rd(ga0, gb0, (t + 1) % 3, 0);
        mm16(ga1, gb1, false);
        SG_HALF16(); PIN_QC(); __builtin_amdgcn_sched_barrier(0);
      }
    }
#pragma unroll
    for (int i = 0; i <= 4; ++i) {
      if (i < 4) {
#pragma unroll
        for (int j = 0; j < 4; ++j) qc[i][j] = MFMA(ga1[i], gb1[j], qc[i][j]);
        asm volatile("" : "+v"(qc[i][0]), "+v"(qc[i][1]), "+v"(qc[i][2]), "+v"(qc[i][3]));
      }
      if (i > 0) {
#pragma unroll
        for (int j = 0; j < 4; ++j)
          *(LAS u32x2*)(size_t)(qa + j * 8192 + (i - 1) * 512) = u32x2{pk(qc[i - 1][j][0], qc[i - 1][j][1]), pk(qc[i - 1][j][2], qc[i - 1][j][3])};
        if (i < 4) { SG_MFMA(1); SG_VALU(2); SG_MFMA(1); SG_VALU(2); SG_DSW(1); SG_MFMA(1); SG_VALU(2); SG_DSW(1); SG_MFMA(1); SG_VALU(2); SG_DSW(2); }
      }
      __builtin_amdgcn_sched_barrier(0);
    }
  }
  ST(2);
  BAR_C();
  ST(3);
  const unsigned want = next_epoch(ep);
  u32x4 pv[2];
  {
    f32x4 sc[4];
    bf16x8 qf[8], kf[8][4];
    auto rdS = [&](int s) {
      qf[s] = ld128(own + s * 1024);
#pragma unroll
      for (int kt = 0; kt < 4; ++kt) kf[s][kt] = ld128(kad[s & 3] + kt * 8192 + (s >> 2) * 256);
      __builtin_amdgcn_sched_barrier(0);
    };
    rdS(0); rdS(1); rdS(2);
#pragma unroll
    for (int s = 0; s < 8; ++s) {
#pragma unroll
      for (int kt = 0; kt < 4; ++kt) sc[kt] = MFMA(kf[s][kt], qf[s], s == 0 ? ZERO4 : sc[kt]);
      asm volatile("" : "+v"(sc[0]), "+v"(sc[1]), "+v"(sc[2]), "+v"(sc[3]));
      __builtin_amdgcn_sched_barrier(0);
      if (s < 5) rdS(s + 3);
    }
    ST(4);
    float mx = max3(max3(sc[0][0], sc[0][1], sc[0][2]), max3(sc[0][3], sc[1][0], sc[1][1]), max3(sc[1][2], sc[1][3], sc[2][0]));
    mx = max3(mx, max3(sc[2][1], sc[2][2], sc[2][3]), max3(max2(sc[3][0], sc[3][1]), sc[3][2], sc[3][3]));
    mx = xmax(mx);
    constexpr float C = 0.0625f * 1.4426950408889634f;
    const float mc = mx * C;
    float l = 0.f;
#pragma unroll
    for (int kt = 0; kt < 4; ++kt)
#pragma unroll
      for (int r = 0; r < 4; ++r) { sc[kt][r] = __builtin_amdgcn_exp2f(fmaf(sc[kt][r], C, -mc)); l += sc[kt][r]; }
    l = xsum(l);
    const float inv = __builtin_amdgcn_rcpf(l);
#pragma unroll
    for (int e = 0; e < 2; ++e) {
      pv[e] = __builtin_bit_cast(u32x4, pk8(sc[2 * e] * inv, sc[2 * e + 1] * inv));
      __builtin_amdgcn_raw_buffer_store_b128(pv[e], rs, pgo + e * 1024, 0, 16);
    }
  }
  ST(5);
  if (m == 7) {
    asm volatile("s_waitcnt vmcnt(0)" ::: "memory");
    if (lane == 0) __hip_atomic_store(pflag_p + m * 4 + w, want, __ATOMIC_RELAXED, __HIP_MEMORY_SCOPE_AGENT);
    STOUT();
    return;
  }
#define FENCE() __builtin_amdgcn_sched_barrier(0)
  f32x4 o[2][4];
  {
    f32x4 acc[4][4];
    bf16x8 fa0[4], fb0[4], fa1[4], fb1[4];
    auto rd = [&](bf16x8 (&fa)[4], bf16x8 (&fb)[4], int slot, int half) {
#pragma unroll
      for (int i = 0; i < 4; ++i) {
        const unsigned lo_ = (slot & 1) * 32768;
        fa[i] = ld128(vba[slot >> 1][half] + lo_ + i * 2048);
        const unsigned wa_ = wba[slot >> 1][i & 1] + lo_ + (i >> 1) * 8192 + half * 4096;
        fb[i] = ldtr(wa_, wa_ + 512);
      }
    };
    auto mm = [&](const bf16x8 (&fa)[4], const bf16x8 (&fb)[4], bool first) {
#pragma unroll
      for (int j = 0; j < 4; ++j)
#pragma unroll
        for (int i = 0; i < 4; ++i) acc[j][i] = MFMA(fa[i], fb[j], first ? ZERO4 : acc[j][i]);
    };
#define PIN_ACC() asm volatile("" : "+v"(acc[0][0]), "+v"(acc[0][1]), "+v"(acc[0][2]), "+v"(acc[0][3]), "+v"(acc[1][0]), "+v"(acc[1][1]), "+v"(acc[1][2]), "+v"(acc[1][3]), \
                                    "+v"(acc[2][0]), "+v"(acc[2][1]), "+v"(acc[2][2]), "+v"(acc[2][3]), "+v"(acc[3][0]), "+v"(acc[3][1]), "+v"(acc[3][2]), "+v"(acc[3][3]))
    bf16x8 cf0[4][2];
    BAR_C();
    ST(6);
    rd(fa0, fb0, zslot(0), 0);
    *(LAS u32x4*)(size_t)plo = pv[0];
    *(LAS u32x4*)(size_t)(plo + 1024) = pv[1];
    FENCE();
#pragma unroll
    for (int z = 0; z < 8; ++z) {
      rd(fa1, fb1, zslot(z), 1);
      mm(fa0, fb0, (z & 3) == 0);
      if (z == 4) asm volatile("" : "+v"(cf0[0][0]), "+v"(cf0[0][1]), "+v"(cf0[1][0]), "+v"(cf0[1][1]), "+v"(cf0[2][0]), "+v"(cf0[2][1]), "+v"(cf0[3][0]), "+v"(cf0[3][1]));
      SG_HALF16();
      PIN_ACC();
      if (z == PFLAG_AT) {
        asm volatile("s_waitcnt vmcnt(0)" ::: "memory");
        if (lane == 0) __hip_atomic_store(pflag_p + m * 4 + w, want, __ATOMIC_RELAXED, __HIP_MEMORY_SCOPE_AGENT);
      }
      BAR_C();
      if (z == 4) ST(8);
      if (z == 5) ST(9);
      if (z == 6) ST(10);
      if (z < 7) {
        rd(fa0, fb0, zslot(z + 1), 0);
        mm(fa1, fb1, false);
        if (z == 3) {
#pragma unroll
          for (int j = 0; j < 4; ++j) { cf0[j][0] = pk8(acc[j][0], acc[j][1]); cf0[j][1] = pk8(acc[j][2], acc[j][3]); }
          SG_MFMA(1); SG_DSR(2); SG_MFMA(1); SG_DSR(2);
#pragma unroll
          for (int i = 0; i < 8; ++i) { SG_MFMA(1); SG_DSR(1); }
#pragma unroll
          for (int i = 0; i < 6; ++i) { SG_MFMA(1); SG_VALU(2); }
          SG_VALU(20);
        } else {
          SG_HALF16();
          PIN_ACC();
        }
        FENCE();
      }
    }
    {
      bf16x8 p1[2][2][4], p2[2][2][4], cf[4][2];
#pragma unroll
      for (int j = 0; j < 4; ++j) {
#pragma unroll
        for (int i = 0; i < 4; ++i) {
          acc[j][i] = MFMA(fa1[i], fb1[j], acc[j][i]);
          asm volatile("" : "+v"(acc[j][i]));
          FENCE();
          p1[j >> 1][j & 1][i] = ld128(pad[i] + (j >> 1) * 8192 + (j & 1) * 1024);
          FENCE();
        }
      }
#pragma unroll
      for (int e = 0; e < 2; ++e)
#pragma unroll
        for (int s = 0; s < 2; ++s) {
          const int j = 2 * e + s;
#pragma unroll
          for (int qt = 0; qt < 4; ++qt) {
#pragma unroll
            for (int nt = 0; nt < 2; ++nt) o[nt][qt] = MFMA(cf0[2 * e + nt][s], p1[e][s][qt], (e == 0 && s == 0) ? ZERO4 : o[nt][qt]);
            p2[e][s][qt] = ld128((pad[qt] ^ 16384) + e * 8192 + s * 1024);
          }
          cf[j][0] = pk8(acc[j][0], acc[j][1]); cf[j][1] = pk8(acc[j][2], acc[j][3]);
#pragma unroll
          for (int i = 0; i < 4; ++i) { SG_MFMA(1); SG_DSR(1); SG_VALU(1); SG_MFMA(1); SG_VALU(2); }
          asm volatile("" : "+v"(o[0][0]), "+v"(o[0][1]), "+v"(o[0][2]), "+v"(o[0][3]), "+v"(o[1][0]), "+v"(o[1][1]), "+v"(o[1][2]), "+v"(o[1][3]), "+v"(cf[j][0]), "+v"(cf[j][1]));
          FENCE();
        }
#pragma unroll
      for (int qt = 3; qt >= 0; --qt) {
#pragma unroll
        for (int e = 0; e < 2; ++e)
#pragma unroll
          for (int s = 0; s < 2; ++s)
#pragma unroll
            for (int nt = 0; nt < 2; ++nt) o[nt][qt] = MFMA(cf[2 * e + nt][s], p2[e][s][qt], o[nt][qt]);
        asm volatile("" : "+v"(o[0][qt]), "+v"(o[1][qt]));
        FENCE();
        if (qt < 3) {
          __builtin_amdgcn_raw_buffer_store_b128(__builtin_bit_cast(u32x4, pk8(o[0][qt + 1], o[1][qt + 1])), rs, fo + ((gp ^ (qt + 1)) << 14), 0, 16);
          FENCE();
        }
      }
    }
  }
  ST(11);
  asm volatile("s_waitcnt vmcnt(0)" ::: "memory");
  if (lane < 4 && lane != gp) __hip_atomic_store(fs, want, __ATOMIC_RELAXED, __HIP_MEMORY_SCOPE_AGENT);
  ST(12);
  {
    for (unsigned spins = 0; spins < SPIN_CAP; ++spins) {
      const unsigned fl = __hip_atomic_load(fp, __ATOMIC_RELAXED, __HIP_MEMORY_SCOPE_AGENT);
      if (__all(fl == want || (lane & 3) == gp)) break;
      __builtin_amdgcn_s_sleep(POLL_SLEEP);
    }
    asm volatile("" ::: "memory");
    ST(13);
    u32x4 d[3];
#pragma unroll
    for (int k = 0; k < 3; ++k) d[k] = __builtin_amdgcn_raw_buffer_load_b128(rs, fi, (gp ^ (k + 1)) * 4096, 16);
    float a[8];
#pragma unroll
    for (int i = 0; i < 4; ++i) { a[i] = o[0][0][i]; a[4 + i] = o[1][0][i]; }
#pragma unroll
    for (int k = 0; k < 3; ++k)
#pragma unroll
      for (int i = 0; i < 4; ++i) { a[2 * i] += __uint_as_float(d[k][i] << 16); a[2 * i + 1] += __uint_as_float(d[k][i] & 0xffff0000u); }
    const auto s0 = __builtin_amdgcn_permlane16_swap(pk(a[0], a[1]), pk(a[4], a[5]), false, false);
    const auto s1 = __builtin_amdgcn_permlane16_swap(pk(a[2], a[3]), pk(a[6], a[7]), false, false);
    *(u32x4*)op = u32x4{s0[0], s1[0], s0[1], s1[1]};
    if (gp == 2 && m == 0 && w == 0 && lane == 0) __hip_atomic_store(epoch_p, want, __ATOMIC_RELAXED, __HIP_MEMORY_SCOPE_AGENT);
  }
  ST(14);
  STOUT();
}

extern "C" void kernel_launch(void* const* d_in, const int* in_sizes, int n_in, void* d_out, int out_size,
                              void* d_ws, size_t ws_size, hipStream_t stream) {
  Params p;
  p.x0 = (const char*)d_in[0]; p.k0 = (const char*)d_in[1]; p.v0 = (const char*)d_in[2]; p.wq0 = (const char*)d_in[3]; p.wo0 = (const char*)d_in[4];
  p.x1 = (const char*)d_in[5]; p.k1 = (const char*)d_in[6]; p.v1 = (const char*)d_in[7]; p.wq1 = (const char*)d_in[8]; p.wo1 = (const char*)d_in[9];
  p.out = (char*)d_out; p.ws = (char*)d_ws;
  gqa_fused<<<256, 512, 0, stream>>>(p);
}
```

```cpp
#include <hip/hip_runtime.h>
#include <hip/hip_bf16.h>

typedef short s16x4 __attribute__((ext_vector_type(4)));
typedef short bf16x8 __attribute__((ext_vector_type(8)));
typedef float f32x4 __attribute__((ext_vector_type(4)));
typedef float f32x2 __attribute__((ext_vector_type(2)));
typedef float f32x16 __attribute__((ext_vector_type(16)));
typedef unsigned u32x4 __attribute__((ext_vector_type(4)));
typedef unsigned u32x2 __attribute__((ext_vector_type(2)));
typedef unsigned u32x16 __attribute__((ext_vector_type(16)));
typedef unsigned u32x8 __attribute__((ext_vector_type(8)));
typedef __bf16 bf16x2_t __attribute__((ext_vector_type(2)));
#define LAS __attribute__((address_space(3)))
#define GAS __attribute__((address_space(1)))
#define CAS __attribute__((address_space(4)))
#define DI __device__ __forceinline__

#define ROT_B 2
#define ROT_H 2
#define POL ""
#define THR 11
#define P_EARLY 0
#define POLL_SLEEP 0
#define A16 1
#define B10_PROMPT 0
#define PIPE_POLL 0
#define PFLAG_AT 1
#define ROT_Z 1

struct Params {
  const char* x0; const char* k0; const char* v0; const char* wq0; const char* wo0;
  const char* x1; const char* k1; const char* v1; const char* wq1; const char* wo1;
  char* out; char* ws;
};

constexpr unsigned WS_EPOCH = 0;
constexpr unsigned WS_PFLAG = 4096;
constexpr unsigned WS_FFLAG = 65536;
constexpr unsigned WS_P = 131072;
constexpr unsigned WS_F = WS_P + 32 * 65536;
constexpr unsigned WS_END = WS_F + 64 * 65536;

constexpr unsigned SW = 0;
constexpr unsigned SX = 131072;

#define ST(i) do {} while (0)
#define STDECL
#define STOUT() do {} while (0)

DI unsigned pk(float a, float b) { f32x2 v = {a, b}; return __builtin_bit_cast(unsigned, __builtin_convertvector(v, bf16x2_t)); }
DI bf16x8 pk8(f32x4 a, f32x4 b) { u32x4 r = {pk(a[0], a[1]), pk(a[2], a[3]), pk(b[0], b[1]), pk(b[2], b[3])}; return __builtin_bit_cast(bf16x8, r); }
DI bf16x8 ld128(unsigned a) { return *(LAS const bf16x8*)(size_t)a; }
DI bf16x8 ldtr(unsigned a0, unsigned a1) {
  s16x4 lo = __builtin_amdgcn_ds_read_tr16_b64_v4i16((LAS s16x4*)(size_t)a0);
  s16x4 hi = __builtin_amdgcn_ds_read_tr16_b64_v4i16((LAS s16x4*)(size_t)a1);
  return __builtin_shufflevector(lo, hi, 0, 1, 2, 3, 4, 5, 6, 7);
}
#define MFMA(a, b, c) __builtin_amdgcn_mfma_f32_16x16x32_bf16((a), (b), (c), 0, 0, 0)
#define MFMA32(a, b, c) __builtin_amdgcn_mfma_f32_32x32x16_bf16((a), (b), (c), 0, 0, 0)
#define ZERO4 (f32x4{0.f, 0.f, 0.f, 0.f})
#define ZERO16 (f32x16{0.f, 0.f, 0.f, 0.f, 0.f, 0.f, 0.f, 0.f, 0.f, 0.f, 0.f, 0.f, 0.f, 0.f, 0.f, 0.f})
DI bf16x8 pk16(const f32x16& x, int a) {
  u32x4 r = {pk(x[8 * a], x[8 * a + 1]), pk(x[8 * a + 2], x[8 * a + 3]), pk(x[8 * a + 4], x[8 * a + 5]), pk(x[8 * a + 6], x[8 * a + 7])};
  return __builtin_bit_cast(bf16x8, r);
}
#define BAR_C() do { __builtin_amdgcn_sched_barrier(0); asm volatile("" ::: "memory"); __builtin_amdgcn_s_waitcnt(0xC07F); \
                     __builtin_amdgcn_s_barrier(); asm volatile("" ::: "memory"); __builtin_amdgcn_sched_barrier(0); } while (0)
#define BAR_L(n) asm volatile("s_waitcnt vmcnt(" #n ")\n\ts_barrier" ::: "memory")
#define SG_MFMA(n) __builtin_amdgcn_sched_group_barrier(0x008, (n), 0)
#define SG_DSR(n) __builtin_amdgcn_sched_group_barrier(0x100, (n), 0)
#define SG_DSW(n) __builtin_amdgcn_sched_group_barrier(0x200, (n), 0)
#define SG_VALU(n) __builtin_amdgcn_sched_group_barrier(0x002, (n), 0)
#define STA(i)
#define STB(i) ST(i)
#define NOLOAD 0
#define GL(n, pol) "s_waitcnt vmcnt(%[thr])\n\tglobal_load_lds_dwordx4 %" #n ", %[sb]" pol "\n\ts_add_u32 m0, m0, 0x1000\n\t"
DI void glds2(const char* sb, unsigned lds_addr, unsigned v0, unsigned v1) {
  if (NOLOAD) return;
  asm volatile("s_mov_b32 m0, %[la]\n\t" GL(0, POL) GL(1, POL) :: "v"(v0), "v"(v1), [sb] "s"(sb), [la] "s"(lds_addr), [thr] "i"(THR) : "memory", "scc");
}
DI void glds4(const char* sb, unsigned lds_addr, const unsigned (&v)[4]) {
  if (NOLOAD) return;
  asm volatile("s_mov_b32 m0, %[la]\n\t" GL(0, POL) GL(1, POL) GL(2, POL) GL(3, POL)
               :: "v"(v[0]), "v"(v[1]), "v"(v[2]), "v"(v[3]), [sb] "s"(sb), [la] "s"(lds_addr), [thr] "i"(THR) : "memory", "scc");
}
DI void glds8(const char* sb, unsigned lds_addr, const unsigned (&v)[8]) {
  if (NOLOAD) return;
  asm volatile("s_mov_b32 m0, %[la]\n\t" GL(0, POL) GL(1, POL) GL(2, POL) GL(3, POL) GL(4, POL) GL(5, POL) GL(6, POL) GL(7, POL)
               :: "v"(v[0]), "v"(v[1]), "v"(v[2]), "v"(v[3]), "v"(v[4]), "v"(v[5]), "v"(v[6]), "v"(v[7]),
                  [sb] "s"(sb), [la] "s"(lds_addr), [thr] "i"(THR) : "memory", "scc");
}
DI void glds2_dev(const char* sb, unsigned lds_addr, unsigned v0, unsigned v1) {
  asm volatile("s_mov_b32 m0, %[la]\n\t" GL(0, " sc1") GL(1, " sc1") :: "v"(v0), "v"(v1), [sb] "s"(sb), [la] "s"(lds_addr), [thr] "i"(THR) : "memory", "scc");
}
DI unsigned next_epoch(unsigned e) { return e * 0x9E3779B1u + 0x7F4A7C15u; }
DI float max2(float a, float b) { return __builtin_elementwise_maximum(a, b); }
DI float max3(float a, float b, float c) { return max2(max2(a, b), c); }
DI float xmax(float v) {
  auto a = __builtin_amdgcn_permlane32_swap(__float_as_uint(v), __float_as_uint(v), false, false);
  v = max2(__uint_as_float(a[0]), __uint_as_float(a[1]));
  auto b = __builtin_amdgcn_permlane16_swap(__float_as_uint(v), __float_as_uint(v), false, false);
  return max2(__uint_as_float(b[0]), __uint_as_float(b[1]));
}
DI float xsum(float v) {
  auto a = __builtin_amdgcn_permlane32_swap(__float_as_uint(v), __float_as_uint(v), false, false);
  v = __uint_as_float(a[0]) + __uint_as_float(a[1]);
  auto b = __builtin_amdgcn_permlane16_swap(__float_as_uint(v), __float_as_uint(v), false, false);
  return __uint_as_float(b[0]) + __uint_as_float(b[1]);
}
constexpr int zslot(int z) { return (0x30213021 >> (4 * z)) & 3; }
constexpr int CODE_BYTES = 21376;
constexpr unsigned SPIN_CAP = 1u << 18;

__global__ __launch_bounds__(512, 2) void gqa_fused(Params P) {
  __shared__ __attribute__((aligned(1024))) char lds[163840];
  u32x16 ka; u32x8 kb8;
  asm volatile("s_load_dwordx16 %0, %2, 0x0\n\ts_load_dwordx8 %1, %2, 0x40" : "=&s"(ka), "=&s"(kb8) : "s"(__builtin_amdgcn_kernarg_segment_ptr()));
  const int tid = threadIdx.x, lane = tid & 63;
  const int w = __builtin_amdgcn_readfirstlane(tid >> 6);
  const int bid = blockIdx.x;
  const int xcd = bid & 7, sl = bid >> 3;
  const int br = xcd >> 2, gp = xcd & 3;
  const int bp = sl >> 3, m = sl & 7;
  const int hj = m & 3, h = gp * 4 + hj, b = bp * 2 + (m >> 2);
  const int team = xcd * 4 + bp, comp = br * 4 + bp, grp = comp * 8 + m;
  const unsigned lds0 = (unsigned)(size_t)(LAS char*)lds;
  const int g1 = ROT_Z ? (bp + m) & 1 : 0;
  const int k1 = ROT_Z == 0 ? 0 : ROT_Z == 1 ? (((bp + m) >> 1) & 1) * 2 + (m >> 2) : ROT_Z == 2 ? ((m >> 1) & 1) * 2 + (m >> 2)
               : ROT_Z == 3 ? (((bp + m) >> 1) & 1) * 2 : ((m >> 1) & 1) * 2;
  STDECL;
  ST(0);
  {
    unsigned long long pc; asm volatile("s_getpc_b64 %0" : "=s"(pc));
    const unsigned vo = min((sl * 8 + w) * 128, CODE_BYTES - 128);
    asm volatile("s_mov_b32 m0, %2\n\ts_nop 0\n\tglobal_load_lds_dword %0, %1" :: "v"(vo), "s"(pc & ~127ull), "s"(lds0 + SX + 3 * 8192 + w * 256) : "memory");
  }
  const int lw = w & 3;
  const unsigned hi = lane >> 5, l31 = lane & 31;
  unsigned v_x = (lane >> 3) * 896 + (((lane & 7) ^ (4 * (lw & 1) + (lane >> 4))) * 16);
  unsigned v_x1 = v_x + 32 * 896;
  unsigned v_wq[8];
#pragma unroll
  for (int i = 0; i < 8; ++i)
    v_wq[i] = hi * 8192 + i * (8 * 8192) + (l31 ^ (((2 * (lw & 1) + hi) | ((i & 1) << 2)) << 1)) * 16;
  int sa = ((ROT_B == 0 ? 0 : ROT_B == 1 ? b : ROT_B == 2 ? (b >> 1) * 2 : ROT_B == 3 ? (b >> 2) * 4 : ROT_B == 4 ? (b & 3) * 2 : (b >> 1) * 3) + ROT_H * hj);
  sa = sa >= 7 ? sa - 7 : sa; sa = sa >= 7 ? sa - 7 : sa; if (ROT_B == 5 || ROT_H > 2) sa = sa >= 7 ? sa - 7 : sa;
  int ox = b * 57344 + lw * (8 * 896), owq = h * 512 + lw * (2 * 8192);
  asm volatile("" : "+v"(v_x), "+v"(v_x1), "+v"(v_wq[0]), "+v"(v_wq[1]), "+v"(v_wq[2]), "+v"(v_wq[3]), "+v"(v_wq[4]), "+v"(v_wq[5]), "+v"(v_wq[6]), "+v"(v_wq[7]));
  asm volatile("s_waitcnt lgkmcnt(0)" : "+s"(ka), "+s"(kb8), "+s"(sa), "+s"(ox), "+s"(owq));
#define KPTR(lo, hi) ((char*)(((unsigned long long)(hi) << 32) | (lo)))
#define K_X (br ? KPTR(ka[10], ka[11]) : KPTR(ka[0], ka[1]))
#define K_K (br ? KPTR(ka[12], ka[13]) : KPTR(ka[2], ka[3]))
#define K_V (br ? KPTR(ka[14], ka[15]) : KPTR(ka[4], ka[5]))
#define K_WQ (br ? KPTR(kb8[0], kb8[1]) : KPTR(ka[6], ka[7]))
#define K_WO (br ? KPTR(kb8[2], kb8[3]) : KPTR(ka[8], ka[9]))
#define K_OUT KPTR(kb8[4], kb8[5])
#define K_WS KPTR(kb8[6], kb8[7])

  if (__builtin_expect(w >= 4, 1)) {
    __builtin_amdgcn_s_setprio(3);
    const char* const xb = K_X + ox;
    const char* const wqb = K_WQ + owq;
    auto stageA = [&](int slot) {
      glds2(xb + sa * 128, lds0 + SX + slot * 8192 + lw * 1024, v_x, v_x1);
      glds8(wqb + sa * (64 * 8192), lds0 + SW + slot * 32768 + lw * 1024, v_wq);
      sa = sa == 6 ? 0 : sa + 1;
    };
    stageA(0);
    char* const ws = K_WS;
    GAS unsigned* const epoch_p = (GAS unsigned*)(ws + WS_EPOCH + comp * 256);
    GAS unsigned* const pflag_p = (GAS unsigned*)(ws + WS_PFLAG + team * 256);
    unsigned epv;
    asm volatile("global_load_dword %0, %1, %2 sc1" : "=v"(epv) : "v"(0), "s"(epoch_p) : "memory");
    unsigned v_k[4], v_wo[4], v_v[4];
#pragma unroll
    for (int i = 0; i < 4; ++i) {
      v_k[i] = ((hi * 512 + ((l31 ^ (2 * lw + hi)) * 16)) ^ ((i & 1) * 128)) + i * 4096;
      v_wo[i] = (lane >> 3) * 896 + (((lane & 7) ^ ((((lane >> 4) & 1) | ((lw & 1) << 1)) << 1)) * 16) + (i >> 1) * 229376 + (i & 1) * (32 * 896);
      v_v[i] = (lane >> 3) * 512 + (((lane & 7) ^ (4 * (lw & 1) + (lane >> 4))) * 16) + (i >> 1) * (8 * 32768) + (i & 1) * 16384;
    }
    const char* const kb = K_K + (b * 8 + (h >> 1)) * 32768 + lw * 1024;
    const char* const vb = K_V + (bp * 16 + gp * 2) * 32768 + lw * 4096;
    const char* const wob = K_WO + gp * (4 * 229376) + m * 128 + lw * (8 * 896);
    auto stageK = [&]() {
      const int f = (h & 1) * 16384;
      glds4(kb + f, lds0 + SW + 3 * 32768 + lw * 1024 + f, v_k);
      glds4(kb + (f ^ 16384), lds0 + SW + 3 * 32768 + lw * 1024 + (f ^ 16384), v_k);
    };
    auto stageZ = [&](int z, int part) {
      const int gl = g1 ^ (z >> 2), kq = (k1 + z) & 3;
      const unsigned d = lds0 + SW + zslot(z) * 32768 + lw * 1024;
      if (part & 1) glds4(wob + gl * (2 * 229376) + kq * (64 * 896), d, v_wo);
      if (part & 2) glds4(vb + gl * 32768 + kq * 128, d + 16384, v_v);
    };
    stageA(1);
    ST(10);
    BAR_L(10);
    asm volatile("" : "+v"(epv));
    const unsigned ep = __builtin_amdgcn_readfirstlane(epv);
    stageA(2);
    ST(1);
#pragma unroll 1
    for (int t = 0; t < 4; ++t) {
      BAR_L(10);
      stageA(t == 3 ? 0 : t);
    }
    BAR_L(10); STA(15); stageK();
    if (m == 7) {
      BAR_L(8); BAR_L(63); BAR_L(0);
      STOUT();
      return;
    }
    BAR_L(8); STA(6); stageZ(0, 3);
    ST(2);
    BAR_L(63);
    BAR_L(8); stageZ(1, 3); stageZ(2, 1);
    ST(3);
    BAR_L(12); stageZ(2, 2); stageZ(3, 1);
    ST(4);
    BAR_L(12); STB(11); stageZ(3, 2); stageZ(4, 1);
    BAR_L(12); STB(12); stageZ(4, 2); stageZ(5, 1);
    ST(5);
    unsigned fl;
    const unsigned voff_f = (lane & 31) * 4;
    const unsigned want = next_epoch(ep);
    auto stageP = [&](int k0, int k1_) {
      const char* src = ws + WS_P + team * 65536 + lw * 1024;
      const unsigned v_lin = lane * 16, v_lin1 = v_lin + 4096;
#pragma unroll
      for (int k = k0; k < k1_; ++k) {
        const int mm = ((m + k) & 3) | ((m ^ k) & 4);
        glds2_dev(src + mm * 8192, lds0 + ((k & 4) ? SW + 1 * 32768 : SX) + (mm & 3) * 8192 + lw * 1024, v_lin, v_lin1);
      }
    };
    BAR_L(12); STB(13); stageZ(5, 2); stageZ(6, 1);
    BAR_L(12); STB(14);
    asm volatile("global_load_dword %0, %1, %2 sc1" : "=v"(fl) : "v"(voff_f), "s"(pflag_p) : "memory");
    stageZ(6, 2); stageZ(7, 1);
    BAR_L(13); STB(15); stageZ(7, 2);
    asm volatile("s_waitcnt vmcnt(12)" : "+v"(fl) :: "memory");
    for (unsigned spins = 0; !__all(fl == want) && spins < SPIN_CAP; ++spins)
      asm volatile("s_sleep 1\n\tglobal_load_dword %0, %1, %2 sc1\n\ts_waitcnt vmcnt(0)" : "=v"(fl) : "v"(voff_f), "s"(pflag_p) : "memory");
    ST(7);
    stageP(1, 3);
    BAR_L(12); stageP(3, 7);
    ST(8);
    BAR_L(12); stageP(7, 8);
    BAR_L(0);
    ST(9);
    STOUT();
    return;
  }

  const unsigned r16 = lane & 15, g = lane >> 4, q4 = r16 >> 2, p4 = lane & 3;
  const unsigned r32 = lane & 31, hh = lane >> 5, blk = g & 1;
  char* const ws = K_WS;
  GAS unsigned* const epoch_p = (GAS unsigned*)(ws + WS_EPOCH + comp * 256);
  GAS unsigned* const pflag_p = (GAS unsigned*)(ws + WS_PFLAG + team * 256);
  const unsigned ep = __builtin_amdgcn_readfirstlane(__hip_atomic_load(epoch_p, __ATOMIC_RELAXED, __HIP_MEMORY_SCOPE_AGENT));
  asm volatile("s_waitcnt vmcnt(0)" ::: "memory");
  const __amdgpu_buffer_rsrc_t rs = __builtin_amdgcn_make_buffer_rsrc(ws, 0, WS_END, 0x00020000);
  const int ct = w & 1, bi = w >> 1;
  unsigned qa = lds0 + SX + w * 2048 + (g >> 1) * 256 + r16 * 16 + (g & 1) * 8;
  unsigned own = lds0 + SX + w * 8192 + lane * 16;
  unsigned kad[4];
#pragma unroll
  for (int i = 0; i < 4; ++i) kad[i] = (lds0 + SW + 3 * 32768 + r16 * 512 + ((g ^ r16) * 16)) ^ (i * 64);
  const unsigned po = w * 2048 + lane * 16;
  unsigned pgo = WS_P + team * 65536 + m * 8192 + po;
  unsigned plo = lds0 + SX + (m & 3) * 8192 + po;
  unsigned wba[2][2], vba[2][2], pad[4];
#pragma unroll
  for (int u = 0; u < 2; ++u)
#pragma unroll
    for (int i = 0; i < 2; ++i) {
      wba[u][i] = lds0 + SW + u * 65536 + (8 * g + q4) * 128 + (((4 * ct + 2 * i + (p4 >> 1)) ^ (((q4 >> 1) | ((g & 1) << 1)) << 1)) * 16) + (p4 & 1) * 8;
      vba[u][i] = lds0 + SW + u * 65536 + 16384 + bi * 8192 + r16 * 128 + (((4 * i + g) ^ ((r16 >> 1) & 7)) * 16);
    }
#pragma unroll
  for (int i = 0; i < 4; ++i) pad[i] = lds0 + (bi != (m >> 2) ? SW + 1 * 32768 : SX) + g1 * 16384 + (gp ^ i) * 2048 + lane * 16;
  unsigned pad2[4];
#pragma unroll
  for (int i = 0; i < 4; ++i) pad2[i] = pad[i] ^ 16384;
  asm volatile("" : "+v"(pad2[0]), "+v"(pad2[1]), "+v"(pad2[2]), "+v"(pad2[3]));
  asm volatile("" : "+v"(wba[0][0]), "+v"(wba[0][1]), "+v"(wba[1][0]), "+v"(wba[1][1]), "+v"(vba[0][0]), "+v"(vba[0][1]), "+v"(vba[1][0]), "+v"(vba[1][1]),
                    "+v"(pad[0]), "+v"(pad[1]), "+v"(pad[2]), "+v"(pad[3]));
  unsigned fo = WS_F + grp * 65536 + gp * 4096 + w * 1024 + lane * 16;
  unsigned fi = WS_F + grp * 65536 + gp * 16384 + w * 1024 + lane * 16;
  GAS unsigned* const fflag_p = (GAS unsigned*)(ws + WS_FFLAG + grp * 1024 + w * 64);
  GAS unsigned* fp = fflag_p + gp * 64 + (lane & 3);
  GAS unsigned* fs = fflag_p + (lane & 3) * 64 + gp;
  char* op = K_OUT + (((bp * 2 + bi) * 64 + gp * 16 + r16) * 896 + br * 448 + m * 64 + ct * 32 + (g & 1) * 16 + (g >> 1) * 8) * 2;
  asm volatile("" : "+v"(fo), "+v"(fi), "+v"(op), "+v"(fp), "+v"(fs));
  asm volatile("" : "+v"(qa), "+v"(own), "+v"(kad[0]), "+v"(kad[1]), "+v"(kad[2]), "+v"(kad[3]), "+v"(pgo), "+v"(plo));
  {
    f32x4 qc[4][4];
    bf16x8 ga0[4], gb0[4], ga1[4], gb1[4];
    const unsigned xa = lds0 + SX + r16 * 128 + ((g ^ ((r16 >> 1) & 7)) * 16);
    const unsigned wa = lds0 + SW + (8 * g + q4) * 512 + (((8 * w + (p4 >> 1)) ^ ((q4 | ((g & 1) << 2)) << 1)) * 16) + (p4 & 1) * 8;
    unsigned xad[2] = {xa, xa ^ 64}, wad[2][4];
#pragma unroll
    for (int i = 0; i < 4; ++i) { wad[0][i] = wa ^ (i * 32); wad[1][i] = wad[0][i] + 65536; }
    asm volatile("" : "+v"(xad[0]), "+v"(xad[1]), "+v"(wad[0][0]), "+v"(wad[0][1]), "+v"(wad[0][2]), "+v"(wad[0][3]),
                      "+v"(wad[1][0]), "+v"(wad[1][1]), "+v"(wad[1][2]), "+v"(wad[1][3]));
    auto rd = [&](bf16x8 (&fa)[4], bf16x8 (&fb)[4], int slot, int half) {
#pragma unroll
      for (int i = 0; i < 4; ++i) {
        const unsigned wo_ = (slot & 1) * 32768 + half * 16384;
        fb[i] = ld128(xad[half] + slot * 8192 + i * 2048);
        fa[i] = ldtr(wad[slot >> 1][i] + wo_, wad[slot >> 1][i] + wo_ + 2048);
      }
    };
    auto mm16 = [&](const bf16x8 (&fa)[4], const bf16x8 (&fb)[4], bool first) {
#pragma unroll
      for (int i = 0; i < 4; ++i)
#pragma unroll
        for (int j = 0; j < 4; ++j) qc[i][j] = MFMA(fa[i], fb[j], first ? ZERO4 : qc[i][j]);
    };
#define SG_HALF16() do { SG_MFMA(1); SG_DSR(2); SG_MFMA(1); SG_DSR(2); _Pragma("unroll") for (int i_ = 0; i_ < 8; ++i_) { SG_MFMA(1); SG_DSR(1); } SG_MFMA(6); } while (0)
#define PIN_QC() asm volatile("" : "+v"(qc[0][0]), "+v"(qc[0][1]), "+v"(qc[0][2]), "+v"(qc[0][3]), "+v"(qc[1][0]), "+v"(qc[1][1]), "+v"(qc[1][2]), "+v"(qc[1][3]), \
                                   "+v"(qc[2][0]), "+v"(qc[2][1]), "+v"(qc[2][2]), "+v"(qc[2][3]), "+v"(qc[3][0]), "+v"(qc[3][1]), "+v"(qc[3][2]), "+v"(qc[3][3]))
    BAR_C();
    ST(1);
    rd(ga0, gb0, 0, 0);
    __builtin_amdgcn_sched_barrier(0);
#pragma unroll
    for (int t = 0; t < 7; ++t) {
      rd(ga1, gb1, t % 3, 1);
      mm16(ga0, gb0, t == 0);
      SG_HALF16();
      PIN_QC();
      BAR_C();
      if (t < 6) {
        rd(ga0, gb0, (t + 1) % 3, 0);
        mm16(ga1, gb1, false);
        SG_HALF16(); PIN_QC(); __builtin_amdgcn_sched_barrier(0);
      }
    }
#pragma unroll
    for (int i = 0; i <= 4; ++i) {
      if (i < 4) {
#pragma unroll
        for (int j = 0; j < 4; ++j) qc[i][j] = MFMA(ga1[i], gb1[j], qc[i][j]);
        asm volatile("" : "+v"(qc[i][0]), "+v"(qc[i][1]), "+v"(qc[i][2]), "+v"(qc[i][3]));
      }
      if (i > 0) {
#pragma unroll
        for (int j = 0; j < 4; ++j)
          *(LAS u32x2*)(size_t)(qa + j * 8192 + (i - 1) * 512) = u32x2{pk(qc[i - 1][j][0], qc[i - 1][j][1]), pk(qc[i - 1][j][2], qc[i - 1][j][3])};
        if (i < 4) { SG_MFMA(1); SG_VALU(2); SG_MFMA(1); SG_VALU(2); SG_DSW(1); SG_MFMA(1); SG_VALU(2); SG_DSW(1); SG_MFMA(1); SG_VALU(2); SG_DSW(2); }
      }
      __builtin_amdgcn_sched_barrier(0);
    }
  }
  ST(2);
  BAR_C();
  ST(3);
  const unsigned want = next_epoch(ep);
  u32x4 pv[2];
  {
    f32x4 sc[4];
    bf16x8 qf[8], kf[8][4];
    auto rdS = [&](int s) {
      qf[s] = ld128(own + s * 1024);
#pragma unroll
      for (int kt = 0; kt < 4; ++kt) kf[s][kt] = ld128(kad[s & 3] + kt * 8192 + (s >> 2) * 256);
      __builtin_amdgcn_sched_barrier(0);
    };
    rdS(0); rdS(1); rdS(2);
#pragma unroll
    for (int s = 0; s < 8; ++s) {
#pragma unroll
      for (int kt = 0; kt < 4; ++kt) sc[kt] = MFMA(kf[s][kt], qf[s], s == 0 ? ZERO4 : sc[kt]);
      asm volatile("" : "+v"(sc[0]), "+v"(sc[1]), "+v"(sc[2]), "+v"(sc[3]));
      __builtin_amdgcn_sched_barrier(0);
      if (s < 5) rdS(s + 3);
    }
    ST(4);
    float mx = max3(max3(sc[0][0], sc[0][1], sc[0][2]), max3(sc[0][3], sc[1][0], sc[1][1]), max3(sc[1][2], sc[1][3], sc[2][0]));
    mx = max3(mx, max3(sc[2][1], sc[2][2], sc[2][3]), max3(max2(sc[3][0], sc[3][1]), sc[3][2], sc[3][3]));
    mx = xmax(mx);
    constexpr float C = 0.0625f * 1.4426950408889634f;
    const float mc = mx * C;
    float l = 0.f;
#pragma unroll
    for (int kt = 0; kt < 4; ++kt)
#pragma unroll
      for (int r = 0; r < 4; ++r) { sc[kt][r] = __builtin_amdgcn_exp2f(fmaf(sc[kt][r], C, -mc)); l += sc[kt][r]; }
    l = xsum(l);
    const float inv = __builtin_amdgcn_rcpf(l);
#pragma unroll
    for (int e = 0; e < 2; ++e) {
      pv[e] = __builtin_bit_cast(u32x4, pk8(sc[2 * e] * inv, sc[2 * e + 1] * inv));
      __builtin_amdgcn_raw_buffer_store_b128(pv[e], rs, pgo + e * 1024, 0, 16);
    }
  }
  ST(5);
  if (m == 7) {
    asm volatile("s_waitcnt vmcnt(0)" ::: "memory");
    if (lane == 0) __hip_atomic_store(pflag_p + m * 4 + w, want, __ATOMIC_RELAXED, __HIP_MEMORY_SCOPE_AGENT);
    STOUT();
    return;
  }
#define FENCE() __builtin_amdgcn_sched_barrier(0)
  f32x4 o[2][4];
  {
    f32x4 acc[4][4];
    bf16x8 fa0[4], fb0[4], fa1[4], fb1[4];
    auto rd = [&](bf16x8 (&fa)[4], bf16x8 (&fb)[4], int slot, int half) {
#pragma unroll
      for (int i = 0; i < 4; ++i) {
        const unsigned lo_ = (slot & 1) * 32768;
        fa[i] = ld128(vba[slot >> 1][half] + lo_ + i * 2048);
        const unsigned wa_ = wba[slot >> 1][i & 1] + lo_ + (i >> 1) * 8192 + half * 4096;
        fb[i] = ldtr(wa_, wa_ + 512);
      }
    };
    auto mm = [&](const bf16x8 (&fa)[4], const bf16x8 (&fb)[4], bool first) {
#pragma unroll
      for (int j = 0; j < 4; ++j)
#pragma unroll
        for (int i = 0; i < 4; ++i) acc[j][i] = MFMA(fa[i], fb[j], first ? ZERO4 : acc[j][i]);
    };
#define PIN_ACC() asm volatile("" : "+v"(acc[0][0]), "+v"(acc[0][1]), "+v"(acc[0][2]), "+v"(acc[0][3]), "+v"(acc[1][0]), "+v"(acc[1][1]), "+v"(acc[1][2]), "+v"(acc[1][3]), \
                                    "+v"(acc[2][0]), "+v"(acc[2][1]), "+v"(acc[2][2]), "+v"(acc[2][3]), "+v"(acc[3][0]), "+v"(acc[3][1]), "+v"(acc[3][2]), "+v"(acc[3][3]))
    bf16x8 cf0[4][2];
    BAR_C();
    ST(6);
    rd(fa0, fb0, zslot(0), 0);
    *(LAS u32x4*)(size_t)plo = pv[0];
    *(LAS u32x4*)(size_t)(plo + 1024) = pv[1];
    FENCE();
#pragma unroll
    for (int z = 0; z < 8; ++z) {
      rd(fa1, fb1, zslot(z), 1);
      mm(fa0, fb0, (z & 3) == 0);
      if (z == 4) asm volatile("" : "+v"(cf0[0][0]), "+v"(cf0[0][1]), "+v"(cf0[1][0]), "+v"(cf0[1][1]), "+v"(cf0[2][0]), "+v"(cf0[2][1]), "+v"(cf0[3][0]), "+v"(cf0[3][1]));
      SG_HALF16();
      PIN_ACC();
      if (z == PFLAG_AT) {
        asm volatile("s_waitcnt vmcnt(0)" ::: "memory");
        if (lane == 0) __hip_atomic_store(pflag_p + m * 4 + w, want, __ATOMIC_RELAXED, __HIP_MEMORY_SCOPE_AGENT);
      }
      BAR_C();
      if (z == 4) ST(8);
      if (z == 5) ST(9);
      if (z == 6) ST(10);
      if (z < 7) {
        rd(fa0, fb0, zslot(z + 1), 0);
        mm(fa1, fb1, false);
        if (z == 3) {
#pragma unroll
          for (int j = 0; j < 4; ++j) { cf0[j][0] = pk8(acc[j][0], acc[j][1]); cf0[j][1] = pk8(acc[j][2], acc[j][3]); }
          SG_MFMA(1); SG_DSR(2); SG_MFMA(1); SG_DSR(2);
#pragma unroll
          for (int i = 0; i < 8; ++i) { SG_MFMA(1); SG_DSR(1); }
#pragma unroll
          for (int i = 0; i < 6; ++i) { SG_MFMA(1); SG_VALU(2); }
          SG_VALU(20);
        } else {
          SG_HALF16();
          PIN_ACC();
        }
        FENCE();
      }
    }
    {
      bf16x8 pf[2][4][2][2], cf[4][2];
      auto rdP = [&](int G, int qt) {
#pragma unroll
        for (int i = 0; i < 4; ++i) pf[G][qt][i >> 1][i & 1] = ld128((G ? pad2[qt] : pad[qt]) + (i >> 1) * 8192 + (i & 1) * 1024);
      };
      auto pc = [&](int G, int qt) {
#pragma unroll
        for (int i = 0; i < 4; ++i)
#pragma unroll
          for (int nt = 0; nt < 2; ++nt)
            o[nt][qt] = MFMA((G ? cf : cf0)[(i & 2) + nt][i & 1], pf[G][qt][i >> 1][i & 1], (G == 0 && i == 0) ? ZERO4 : o[nt][qt]);
      };
      auto pin = [&](int qt) { asm volatile("" : "+v"(o[0][qt]), "+v"(o[1][qt])); FENCE(); };
      auto send = [&](int qt) {
        __builtin_amdgcn_raw_buffer_store_b128(__builtin_bit_cast(u32x4, pk8(o[0][qt], o[1][qt])), rs, fo + ((gp ^ qt) << 14), 0, 16);
        FENCE();
      };
#define SG_PC(nr, nv) do { _Pragma("unroll") for (int i_ = 0; i_ < 4; ++i_) { SG_MFMA(1); if (nr) SG_DSR(1); SG_MFMA(1); if (nv) SG_VALU(nv); } } while (0)
#pragma unroll
      for (int j = 0; j < 4; ++j) {
#pragma unroll
        for (int i = 0; i < 4; ++i) acc[j][i] = MFMA(fa1[i], fb1[j], acc[j][i]);
        if (j == 0) rdP(0, 3);
        if (j == 1) rdP(0, 2);
        if (j == 2) rdP(1, 3);
        if (j == 3) rdP(1, 2);
#pragma unroll
        for (int i = 0; i < 4; ++i) { SG_MFMA(1); SG_DSR(1); }
        asm volatile("" : "+v"(acc[j][0]), "+v"(acc[j][1]), "+v"(acc[j][2]), "+v"(acc[j][3]));
        FENCE();
      }
      pc(0, 3); rdP(0, 1);
      cf[0][0] = pk8(acc[0][0], acc[0][1]); cf[0][1] = pk8(acc[0][2], acc[0][3]); cf[1][0] = pk8(acc[1][0], acc[1][1]); cf[1][1] = pk8(acc[1][2], acc[1][3]);
      SG_PC(1, 4);
      asm volatile("" : "+v"(cf[0][0]), "+v"(cf[0][1]), "+v"(cf[1][0]), "+v"(cf[1][1])); pin(3);
      pc(0, 2); rdP(1, 1);
      cf[2][0] = pk8(acc[2][0], acc[2][1]); cf[2][1] = pk8(acc[2][2], acc[2][3]); cf[3][0] = pk8(acc[3][0], acc[3][1]); cf[3][1] = pk8(acc[3][2], acc[3][3]);
      SG_PC(1, 4);
      asm volatile("" : "+v"(cf[2][0]), "+v"(cf[2][1]), "+v"(cf[3][0]), "+v"(cf[3][1])); pin(2);
      pc(1, 3); rdP(0, 0); SG_PC(1, 0); pin(3);
      pc(1, 2); rdP(1, 0); SG_PC(1, 0); pin(2);
      send(3);
      pc(0, 1); pin(1);
      send(2);
      pc(1, 1); pin(1);
      pc(0, 0); pin(0);
      send(1);
      pc(1, 0); pin(0);
    }
  }
  ST(11);
  asm volatile("s_waitcnt vmcnt(0)" ::: "memory");
  if (lane < 4 && lane != gp) __hip_atomic_store(fs, want, __ATOMIC_RELAXED, __HIP_MEMORY_SCOPE_AGENT);
  ST(12);
  {
    u32x4 d[3];
    for (unsigned spins = 0; spins < SPIN_CAP; ++spins) {
      const unsigned fl = __hip_atomic_load(fp, __ATOMIC_RELAXED, __HIP_MEMORY_SCOPE_AGENT);
      if (__all(fl == want || (lane & 3) == gp)) break;
      __builtin_amdgcn_s_sleep(POLL_SLEEP);
    }
    asm volatile("" ::: "memory");
    ST(13);
#pragma unroll
    for (int k = 0; k < 3; ++k) d[k] = __builtin_amdgcn_raw_buffer_load_b128(rs, fi, (gp ^ (k + 1)) * 4096, 16);
    float a[8];
#pragma unroll
    for (int i = 0; i < 4; ++i) { a[i] = o[0][0][i]; a[4 + i] = o[1][0][i]; }
#pragma unroll
    for (int k = 0; k < 3; ++k)
#pragma unroll
      for (int i = 0; i < 4; ++i) { a[2 * i] += __uint_as_float(d[k][i] << 16); a[2 * i + 1] += __uint_as_float(d[k][i] & 0xffff0000u); }
    const auto s0 = __builtin_amdgcn_permlane16_swap(pk(a[0], a[1]), pk(a[4], a[5]), false, false);
    const auto s1 = __builtin_amdgcn_permlane16_swap(pk(a[2], a[3]), pk(a[6], a[7]), false, false);
    *(u32x4*)op = u32x4{s0[0], s1[0], s0[1], s1[1]};
    if (gp == 2 && m == 0 && w == 0 && lane == 0) __hip_atomic_store(epoch_p, want, __ATOMIC_RELAXED, __HIP_MEMORY_SCOPE_AGENT);
  }
  ST(14);
  STOUT();
}

extern "C" void kernel_launch(void* const* d_in, const int* in_sizes, int n_in, void* d_out, int out_size,
                              void* d_ws, size_t ws_size, hipStream_t stream) {
  Params p;
  p.x0 = (const char*)d_in[0]; p.k0 = (const char*)d_in[1]; p.v0 = (const char*)d_in[2]; p.wq0 = (const char*)d_in[3]; p.wo0 = (const char*)d_in[4];
  p.x1 = (const char*)d_in[5]; p.k1 = (const char*)d_in[6]; p.v1 = (const char*)d_in[7]; p.wq1 = (const char*)d_in[8]; p.wo1 = (const char*)d_in[9];
  p.out = (char*)d_out; p.ws = (char*)d_ws;
  gqa_fused<<<256, 512, 0, stream>>>(p);
}
```

```cpp
#include <hip/hip_runtime.h>
#include <hip/hip_bf16.h>

typedef short s16x4 __attribute__((ext_vector_type(4)));
typedef short bf16x8 __attribute__((ext_vector_type(8)));
typedef float f32x4 __attribute__((ext_vector_type(4)));
typedef float f32x2 __attribute__((ext_vector_type(2)));
typedef float f32x16 __attribute__((ext_vector_type(16)));
typedef unsigned u32x4 __attribute__((ext_vector_type(4)));
typedef unsigned u32x2 __attribute__((ext_vector_type(2)));
typedef unsigned u32x16 __attribute__((ext_vector_type(16)));
typedef unsigned u32x8 __attribute__((ext_vector_type(8)));
typedef __bf16 bf16x2_t __attribute__((ext_vector_type(2)));
#define LAS __attribute__((address_space(3)))
#define GAS __attribute__((address_space(1)))
#define CAS __attribute__((address_space(4)))
#define DI __device__ __forceinline__

#define ROT_B 2
#define ROT_H 2
#define POL ""
#define THR 11
#define P_EARLY 0
#define POLL_SLEEP 0
#define A16 1
#define B10_PROMPT 0
#define PIPE_POLL 0
#define TOUCH_Z -1
#define TOUCH_K -1
#define TOUCH_ST 0
#define TOUCH_W1 1
#define ROT_Z2 0
#define PFLAG_AT 1
#define ROT_Z 7

struct Params {
  const char* x0; const char* k0; const char* v0; const char* wq0; const char* wo0;
  const char* x1; const char* k1; const char* v1; const char* wq1; const char* wo1;
  char* out; char* ws;
};

constexpr unsigned WS_EPOCH = 0;
constexpr unsigned WS_PFLAG = 4096;
constexpr unsigned WS_FFLAG = 65536;
constexpr unsigned WS_P = 131072;
constexpr unsigned WS_F = WS_P + 32 * 65536;
constexpr unsigned WS_END = WS_F + 64 * 65536;

constexpr unsigned SW = 0;
constexpr unsigned SX = 131072;

#define ST(i) do {} while (0)
#define STDECL
#define STOUT() do {} while (0)

DI unsigned pk(float a, float b) { f32x2 v = {a, b}; return __builtin_bit_cast(unsigned, __builtin_convertvector(v, bf16x2_t)); }
DI bf16x8 pk8(f32x4 a, f32x4 b) { u32x4 r = {pk(a[0], a[1]), pk(a[2], a[3]), pk(b[0], b[1]), pk(b[2], b[3])}; return __builtin_bit_cast(bf16x8, r); }
DI bf16x8 ld128(unsigned a) { return *(LAS const bf16x8*)(size_t)a; }
DI bf16x8 ldtr(unsigned a0, unsigned a1) {
  s16x4 lo = __builtin_amdgcn_ds_read_tr16_b64_v4i16((LAS s16x4*)(size_t)a0);
  s16x4 hi = __builtin_amdgcn_ds_read_tr16_b64_v4i16((LAS s16x4*)(size_t)a1);
  return __builtin_shufflevector(lo, hi, 0, 1, 2, 3, 4, 5, 6, 7);
}
#define MFMA(a, b, c) __builtin_amdgcn_mfma_f32_16x16x32_bf16((a), (b), (c), 0, 0, 0)
#define MFMA32(a, b, c) __builtin_amdgcn_mfma_f32_32x32x16_bf16((a), (b), (c), 0, 0, 0)
#define ZERO4 (f32x4{0.f, 0.f, 0.f, 0.f})
#define ZERO16 (f32x16{0.f, 0.f, 0.f, 0.f, 0.f, 0.f, 0.f, 0.f, 0.f, 0.f, 0.f, 0.f, 0.f, 0.f, 0.f, 0.f})
DI bf16x8 pk16(const f32x16& x, int a) {
  u32x4 r = {pk(x[8 * a], x[8 * a + 1]), pk(x[8 * a + 2], x[8 * a + 3]), pk(x[8 * a + 4], x[8 * a + 5]), pk(x[8 * a + 6], x[8 * a + 7])};
  return __builtin_bit_cast(bf16x8, r);
}
#define BAR_C() do { __builtin_amdgcn_sched_barrier(0); asm volatile("" ::: "memory"); __builtin_amdgcn_s_waitcnt(0xC07F); \
                     __builtin_amdgcn_s_barrier(); asm volatile("" ::: "memory"); __builtin_amdgcn_sched_barrier(0); } while (0)
#define BAR_L(n) asm volatile("s_waitcnt vmcnt(" #n ")\n\ts_barrier" ::: "memory")
#define SG_MFMA(n) __builtin_amdgcn_sched_group_barrier(0x008, (n), 0)
#define SG_DSR(n) __builtin_amdgcn_sched_group_barrier(0x100, (n), 0)
#define SG_DSW(n) __builtin_amdgcn_sched_group_barrier(0x200, (n), 0)
#define SG_VALU(n) __builtin_amdgcn_sched_group_barrier(0x002, (n), 0)
#define STA(i)
#define STB(i) ST(i)
#define NOLOAD 0
#define GL(n, pol) "s_waitcnt vmcnt(%[thr])\n\tglobal_load_lds_dwordx4 %" #n ", %[sb]" pol "\n\ts_add_u32 m0, m0, 0x1000\n\t"
DI void glds2(const char* sb, unsigned lds_addr, unsigned v0, unsigned v1) {
  if (NOLOAD) return;
  asm volatile("s_mov_b32 m0, %[la]\n\t" GL(0, POL) GL(1, POL) :: "v"(v0), "v"(v1), [sb] "s"(sb), [la] "s"(lds_addr), [thr] "i"(THR) : "memory", "scc");
}
DI void glds4(const char* sb, unsigned lds_addr, const unsigned (&v)[4]) {
  if (NOLOAD) return;
  asm volatile("s_mov_b32 m0, %[la]\n\t" GL(0, POL) GL(1, POL) GL(2, POL) GL(3, POL)
               :: "v"(v[0]), "v"(v[1]), "v"(v[2]), "v"(v[3]), [sb] "s"(sb), [la] "s"(lds_addr), [thr] "i"(THR) : "memory", "scc");
}
DI void glds8(const char* sb, unsigned lds_addr, const unsigned (&v)[8]) {
  if (NOLOAD) return;
  asm volatile("s_mov_b32 m0, %[la]\n\t" GL(0, POL) GL(1, POL) GL(2, POL) GL(3, POL) GL(4, POL) GL(5, POL) GL(6, POL) GL(7, POL)
               :: "v"(v[0]), "v"(v[1]), "v"(v[2]), "v"(v[3]), "v"(v[4]), "v"(v[5]), "v"(v[6]), "v"(v[7]),
                  [sb] "s"(sb), [la] "s"(lds_addr), [thr] "i"(THR) : "memory", "scc");
}
DI void glds2_dev(const char* sb, unsigned lds_addr, unsigned v0, unsigned v1) {
  asm volatile("s_mov_b32 m0, %[la]\n\t" GL(0, " sc1") GL(1, " sc1") :: "v"(v0), "v"(v1), [sb] "s"(sb), [la] "s"(lds_addr), [thr] "i"(THR) : "memory", "scc");
}
DI unsigned next_epoch(unsigned e) { return e * 0x9E3779B1u + 0x7F4A7C15u; }
DI float max2(float a, float b) { return __builtin_elementwise_maximum(a, b); }
DI float max3(float a, float b, float c) { return max2(max2(a, b), c); }
DI float xmax(float v) {
  auto a = __builtin_amdgcn_permlane32_swap(__float_as_uint(v), __float_as_uint(v), false, false);
  v = max2(__uint_as_float(a[0]), __uint_as_float(a[1]));
  auto b = __builtin_amdgcn_permlane16_swap(__float_as_uint(v), __float_as_uint(v), false, false);
  return max2(__uint_as_float(b[0]), __uint_as_float(b[1]));
}
DI float xsum(float v) {
  auto a = __builtin_amdgcn_permlane32_swap(__float_as_uint(v), __float_as_uint(v), false, false);
  v = __uint_as_float(a[0]) + __uint_as_float(a[1]);
  auto b = __builtin_amdgcn_permlane16_swap(__float_as_uint(v), __float_as_uint(v), false, false);
  return __uint_as_float(b[0]) + __uint_as_float(b[1]);
}
constexpr int zslot(int z) { return (0x30213021 >> (4 * z)) & 3; }
constexpr int CODE_BYTES = 21376;
constexpr unsigned SPIN_CAP = 1u << 18;

__global__ __launch_bounds__(512, 2) void gqa_fused(Params P) {
  __shared__ __attribute__((aligned(1024))) char lds[163840];
  u32x16 ka; u32x8 kb8;
  asm volatile("s_load_dwordx16 %0, %2, 0x0\n\ts_load_dwordx8 %1, %2, 0x40" : "=&s"(ka), "=&s"(kb8) : "s"(__builtin_amdgcn_kernarg_segment_ptr()));
  const int tid = threadIdx.x, lane = tid & 63;
  const int w = __builtin_amdgcn_readfirstlane(tid >> 6);
  const int bid = blockIdx.x;
  const int xcd = bid & 7, sl = bid >> 3;
  const int br = xcd >> 2, gp = xcd & 3;
  const int bp = sl >> 3, m = sl & 7;
  const int hj = m & 3, h = gp * 4 + hj, b = bp * 2 + (m >> 2);
  const int team = xcd * 4 + bp, comp = br * 4 + bp, grp = comp * 8 + m;
  const unsigned lds0 = (unsigned)(size_t)(LAS char*)lds;
  const int g1 = ROT_Z == 0 || ROT_Z == 5 || ROT_Z == 7 ? 0 : (bp + m) & 1;
  const int k1 = ROT_Z == 0 ? 0 : ROT_Z == 1 ? (((bp + m) >> 1) & 1) * 2 + (m >> 2) : ROT_Z == 2 ? ((m >> 1) & 1) * 2 + (m >> 2)
               : ROT_Z == 3 ? (((bp + m) >> 1) & 1) * 2 : ROT_Z == 4 ? ((m >> 1) & 1) * 2 : ROT_Z == 5 ? ((bp + m) & 1) * 2 : ROT_Z == 6 ? 0 : (bp + m) & 3;
  const int k2 = ROT_Z2 == 0 ? k1 : ROT_Z2 == 1 ? (bp + m) & 3 : ((bp + m) & 1) * 2;
  STDECL;
  ST(0);
  {
    unsigned long long pc; asm volatile("s_getpc_b64 %0" : "=s"(pc));
    const unsigned vo = min((sl * 8 + w) * 128, CODE_BYTES - 128);
    asm volatile("s_mov_b32 m0, %2\n\ts_nop 0\n\tglobal_load_lds_dword %0, %1" :: "v"(vo), "s"(pc & ~127ull), "s"(lds0 + SX + 3 * 8192 + w * 256) : "memory");
  }
  const int lw = w & 3;
  const unsigned hi = lane >> 5, l31 = lane & 31;
  unsigned v_x = (lane >> 3) * 896 + (((lane & 7) ^ (4 * (lw & 1) + (lane >> 4))) * 16);
  unsigned v_x1 = v_x + 32 * 896;
  unsigned v_wq[8];
#pragma unroll
  for (int i = 0; i < 8; ++i)
    v_wq[i] = hi * 8192 + i * (8 * 8192) + (l31 ^ (((2 * (lw & 1) + hi) | ((i & 1) << 2)) << 1)) * 16;
  int sa = ((ROT_B == 0 ? 0 : ROT_B == 1 ? b : ROT_B == 2 ? (b >> 1) * 2 : ROT_B == 3 ? (b >> 2) * 4 : ROT_B == 4 ? (b & 3) * 2 : (b >> 1) * 3) + ROT_H * hj);
  sa = sa >= 7 ? sa - 7 : sa; sa = sa >= 7 ? sa - 7 : sa; if (ROT_B == 5 || ROT_H > 2) sa = sa >= 7 ? sa - 7 : sa;
  int ox = b * 57344 + lw * (8 * 896), owq = h * 512 + lw * (2 * 8192);
  asm volatile("" : "+v"(v_x), "+v"(v_x1), "+v"(v_wq[0]), "+v"(v_wq[1]), "+v"(v_wq[2]), "+v"(v_wq[3]), "+v"(v_wq[4]), "+v"(v_wq[5]), "+v"(v_wq[6]), "+v"(v_wq[7]));
  asm volatile("s_waitcnt lgkmcnt(0)" : "+s"(ka), "+s"(kb8), "+s"(sa), "+s"(ox), "+s"(owq));
#define KPTR(lo, hi) ((char*)(((unsigned long long)(hi) << 32) | (lo)))
#define K_X (br ? KPTR(ka[10], ka[11]) : KPTR(ka[0], ka[1]))
#define K_K (br ? KPTR(ka[12], ka[13]) : KPTR(ka[2], ka[3]))
#define K_V (br ? KPTR(ka[14], ka[15]) : KPTR(ka[4], ka[5]))
#define K_WQ (br ? KPTR(kb8[0], kb8[1]) : KPTR(ka[6], ka[7]))
#define K_WO (br ? KPTR(kb8[2], kb8[3]) : KPTR(ka[8], ka[9]))
#define K_OUT KPTR(kb8[4], kb8[5])
#define K_WS KPTR(kb8[6], kb8[7])

  if (__builtin_expect(w >= 4, 1)) {
    __builtin_amdgcn_s_setprio(3);
    const char* const xb = K_X + ox;
    const char* const wqb = K_WQ + owq;
    auto stageA = [&](int slot) {
      glds2(xb + sa * 128, lds0 + SX + slot * 8192 + lw * 1024, v_x, v_x1);
      glds8(wqb + sa * (64 * 8192), lds0 + SW + slot * 32768 + lw * 1024, v_wq);
      sa = sa == 6 ? 0 : sa + 1;
    };
    stageA(0);
    char* const ws = K_WS;
    GAS unsigned* const epoch_p = (GAS unsigned*)(ws + WS_EPOCH + comp * 256);
    GAS unsigned* const pflag_p = (GAS unsigned*)(ws + WS_PFLAG + team * 256);
    unsigned epv;
    unsigned v_k[4], v_wo[4], v_v[4];
#pragma unroll
    for (int i = 0; i < 4; ++i) {
      v_k[i] = ((hi * 512 + ((l31 ^ (2 * lw + hi)) * 16)) ^ ((i & 1) * 128)) + i * 4096;
      v_wo[i] = (lane >> 3) * 896 + (((lane & 7) ^ ((((lane >> 4) & 1) | ((lw & 1) << 1)) << 1)) * 16) + (i >> 1) * 229376 + (i & 1) * (32 * 896);
      v_v[i] = (lane >> 3) * 512 + (((lane & 7) ^ (4 * (lw & 1) + (lane >> 4))) * 16) + (i >> 1) * (8 * 32768) + (i & 1) * 16384;
    }
    const char* const kb = K_K + (b * 8 + (h >> 1)) * 32768 + lw * 1024;
    const char* const vb = K_V + (bp * 16 + gp * 2) * 32768 + lw * 4096;
    const char* const wob = K_WO + gp * (4 * 229376) + m * 128 + lw * (8 * 896);
    auto stageK = [&]() {
      const int f = (h & 1) * 16384;
      glds4(kb + f, lds0 + SW + 3 * 32768 + lw * 1024 + f, v_k);
      glds4(kb + (f ^ 16384), lds0 + SW + 3 * 32768 + lw * 1024 + (f ^ 16384), v_k);
    };
    auto stageZ = [&](int z, int part) {
      const int gl = g1 ^ (z >> 2), kq = ((z < 4 ? k1 : k2) + z) & 3;
      const unsigned d = lds0 + SW + zslot(z) * 32768 + lw * 1024;
      if (part & 1) glds4(wob + gl * (2 * 229376) + kq * (64 * 896), d, v_wo);
      if (part & 2) glds4(vb + gl * 32768 + kq * 128, d + 16384, v_v);
    };
    stageA(1);
    ST(10);
    BAR_L(10);
    asm volatile("global_load_dword %0, %1, %2 sc1" : "=v"(epv) : "v"(0), "s"(epoch_p) : "memory");
    stageA(2);
    ST(1);
    auto touch = [&](const char* base, unsigned stride) {
      asm volatile("s_mov_b32 m0, %2\n\ts_nop 0\n\tglobal_load_lds_dword %0, %1" :: "v"(lane * stride), "s"(base), "s"(lds0 + SX + 3 * 8192 + 2048 + lw * 256) : "memory");
    };
    auto touchZ0 = [&]() {
      if (lw < 2) touch(wob - lw * (8 * 896) + (g1 * 2 + lw) * 229376 + k1 * (64 * 896), 896);
      else touch(vb - lw * 4096 + g1 * 32768 + (lw - 2) * (8 * 32768) + k1 * 128, 512);
    };
    auto touchK = [&]() {
      if (lw < 2) touch(kb - lw * 1024 + (h & 1) * 16384 + lw * 8192, 128);
      else if (TOUCH_W1) touch(wob - lw * (8 * 896) + (g1 * 2 + lw - 2) * 229376 + ((k1 + 1) & 3) * (64 * 896), 896);
    };
    const int tz = TOUCH_Z < 0 || m == 7 ? -1 : TOUCH_Z + (TOUCH_ST ? (bp + m) % TOUCH_ST : 0);
    const int tk = TOUCH_K < 0 ? -1 : TOUCH_K + (TOUCH_ST ? (bp + m + 1) % TOUCH_ST : 0);
#pragma unroll 1
    for (int t = 0; t < 4; ++t) {
      BAR_L(10);
      stageA(t == 3 ? 0 : t);
      if (t == tz) touchZ0();
      if (t == tk) touchK();
    }
    asm volatile("" : "+v"(epv));
    const unsigned ep = __builtin_amdgcn_readfirstlane(epv);
    BAR_L(10); STA(15); stageK();
    if (m == 7) {
      BAR_L(8); BAR_L(63); BAR_L(0);
      STOUT();
      return;
    }
    BAR_L(8); STA(6); stageZ(0, 3);
    ST(2);
    BAR_L(63);
    BAR_L(8); stageZ(1, 3); stageZ(2, 1);
    ST(3);
    BAR_L(12); stageZ(2, 2); stageZ(3, 1);
    ST(4);
    BAR_L(12); STB(11); stageZ(3, 2); stageZ(4, 1);
    BAR_L(12); STB(12); stageZ(4, 2); stageZ(5, 1);
    ST(5);
    unsigned fl;
    const unsigned voff_f = (lane & 31) * 4;
    const unsigned want = next_epoch(ep);
    auto stageP = [&](int k0, int k1_) {
      const char* src = ws + WS_P + team * 65536 + lw * 1024;
      const unsigned v_lin = lane * 16, v_lin1 = v_lin + 4096;
#pragma unroll
      for (int k = k0; k < k1_; ++k) {
        const int mm = ((m + k) & 3) | ((m ^ k) & 4);
        glds2_dev(src + mm * 8192, lds0 + ((k & 4) ? SW + 1 * 32768 : SX) + (mm & 3) * 8192 + lw * 1024, v_lin, v_lin1);
      }
    };
    BAR_L(12); STB(13); stageZ(5, 2); stageZ(6, 1);
    BAR_L(12); STB(14);
    asm volatile("global_load_dword %0, %1, %2 sc1" : "=v"(fl) : "v"(voff_f), "s"(pflag_p) : "memory");
    stageZ(6, 2); stageZ(7, 1);
    BAR_L(13); STB(15); stageZ(7, 2);
    asm volatile("s_waitcnt vmcnt(12)" : "+v"(fl) :: "memory");
    for (unsigned spins = 0; !__all(fl == want) && spins < SPIN_CAP; ++spins)
      asm volatile("s_sleep 1\n\tglobal_load_dword %0, %1, %2 sc1\n\ts_waitcnt vmcnt(0)" : "=v"(fl) : "v"(voff_f), "s"(pflag_p) : "memory");
    ST(7);
    stageP(1, 3);
    BAR_L(12); stageP(3, 7);
    ST(8);
    BAR_L(12); stageP(7, 8);
    BAR_L(0);
    ST(9);
    STOUT();
    return;
  }

  const unsigned r16 = lane & 15, g = lane >> 4, q4 = r16 >> 2, p4 = lane & 3;
  const unsigned r32 = lane & 31, hh = lane >> 5, blk = g & 1;
  char* const ws = K_WS;
  GAS unsigned* const epoch_p = (GAS unsigned*)(ws + WS_EPOCH + comp * 256);
  GAS unsigned* const pflag_p = (GAS unsigned*)(ws + WS_PFLAG + team * 256);
  asm volatile("s_waitcnt vmcnt(0)" ::: "memory");
  const __amdgpu_buffer_rsrc_t rs = __builtin_amdgcn_make_buffer_rsrc(ws, 0, WS_END, 0x00020000);
  const int ct = w & 1, bi = w >> 1;
  unsigned qa = lds0 + SX + w * 2048 + (g >> 1) * 256 + r16 * 16 + (g & 1) * 8;
  unsigned own = lds0 + SX + w * 8192 + lane * 16;
  unsigned kad[4];
#pragma unroll
  for (int i = 0; i < 4; ++i) kad[i] = (lds0 + SW + 3 * 32768 + r16 * 512 + ((g ^ r16) * 16)) ^ (i * 64);
  const unsigned po = w * 2048 + lane * 16;
  unsigned pgo = WS_P + team * 65536 + m * 8192 + po;
  unsigned plo = lds0 + SX + (m & 3) * 8192 + po;
  unsigned wba[2][2], vba[2][2], pad[4];
#pragma unroll
  for (int u = 0; u < 2; ++u)
#pragma unroll
    for (int i = 0; i < 2; ++i) {
      wba[u][i] = lds0 + SW + u * 65536 + (8 * g + q4) * 128 + (((4 * ct + 2 * i + (p4 >> 1)) ^ (((q4 >> 1) | ((g & 1) << 1)) << 1)) * 16) + (p4 & 1) * 8;
      vba[u][i] = lds0 + SW + u * 65536 + 16384 + bi * 8192 + r16 * 128 + (((4 * i + g) ^ ((r16 >> 1) & 7)) * 16);
    }
#pragma unroll
  for (int i = 0; i < 4; ++i) pad[i] = lds0 + (bi != (m >> 2) ? SW + 1 * 32768 : SX) + g1 * 16384 + (gp ^ i) * 2048 + lane * 16;
  unsigned pad2[4];
#pragma unroll
  for (int i = 0; i < 4; ++i) pad2[i] = pad[i] ^ 16384;
  asm volatile("" : "+v"(pad2[0]), "+v"(pad2[1]), "+v"(pad2[2]), "+v"(pad2[3]));
  asm volatile("" : "+v"(wba[0][0]), "+v"(wba[0][1]), "+v"(wba[1][0]), "+v"(wba[1][1]), "+v"(vba[0][0]), "+v"(vba[0][1]), "+v"(vba[1][0]), "+v"(vba[1][1]),
                    "+v"(pad[0]), "+v"(pad[1]), "+v"(pad[2]), "+v"(pad[3]));
  unsigned fo = WS_F + grp * 65536 + gp * 4096 + w * 1024 + lane * 16;
  unsigned fi = WS_F + grp * 65536 + gp * 16384 + w * 1024 + lane * 16;
  GAS unsigned* const fflag_p = (GAS unsigned*)(ws + WS_FFLAG + grp * 1024 + w * 64);
  GAS unsigned* fp = fflag_p + gp * 64 + (lane & 3);
  GAS unsigned* fs = fflag_p + (lane & 3) * 64 + gp;
  char* op = K_OUT + (((bp * 2 + bi) * 64 + gp * 16 + r16) * 896 + br * 448 + m * 64 + ct * 32 + (g & 1) * 16 + (g >> 1) * 8) * 2;
  asm volatile("" : "+v"(fo), "+v"(fi), "+v"(op), "+v"(fp), "+v"(fs));
  asm volatile("" : "+v"(qa), "+v"(own), "+v"(kad[0]), "+v"(kad[1]), "+v"(kad[2]), "+v"(kad[3]), "+v"(pgo), "+v"(plo));
  unsigned epv;
  {
    f32x4 qc[4][4];
    bf16x8 ga0[4], gb0[4], ga1[4], gb1[4];
    const unsigned xa = lds0 + SX + r16 * 128 + ((g ^ ((r16 >> 1) & 7)) * 16);
    const unsigned wa = lds0 + SW + (8 * g + q4) * 512 + (((8 * w + (p4 >> 1)) ^ ((q4 | ((g & 1) << 2)) << 1)) * 16) + (p4 & 1) * 8;
    unsigned xad[2] = {xa, xa ^ 64}, wad[2][4];
#pragma unroll
    for (int i = 0; i < 4; ++i) { wad[0][i] = wa ^ (i * 32); wad[1][i] = wad[0][i] + 65536; }
    asm volatile("" : "+v"(xad[0]), "+v"(xad[1]), "+v"(wad[0][0]), "+v"(wad[0][1]), "+v"(wad[0][2]), "+v"(wad[0][3]),
                      "+v"(wad[1][0]), "+v"(wad[1][1]), "+v"(wad[1][2]), "+v"(wad[1][3]));
    auto rd = [&](bf16x8 (&fa)[4], bf16x8 (&fb)[4], int slot, int half) {
#pragma unroll
      for (int i = 0; i < 4; ++i) {
        const unsigned wo_ = (slot & 1) * 32768 + half * 16384;
        fb[i] = ld128(xad[half] + slot * 8192 + i * 2048);
        fa[i] = ldtr(wad[slot >> 1][i] + wo_, wad[slot >> 1][i] + wo_ + 2048);
      }
    };
    auto mm16 = [&](const bf16x8 (&fa)[4], const bf16x8 (&fb)[4], bool first) {
#pragma unroll
      for (int i = 0; i < 4; ++i)
#pragma unroll
        for (int j = 0; j < 4; ++j) qc[i][j] = MFMA(fa[i], fb[j], first ? ZERO4 : qc[i][j]);
    };
#define SG_HALF16() do { SG_MFMA(1); SG_DSR(2); SG_MFMA(1); SG_DSR(2); _Pragma("unroll") for (int i_ = 0; i_ < 8; ++i_) { SG_MFMA(1); SG_DSR(1); } SG_MFMA(6); } while (0)
#define PIN_QC() asm volatile("" : "+v"(qc[0][0]), "+v"(qc[0][1]), "+v"(qc[0][2]), "+v"(qc[0][3]), "+v"(qc[1][0]), "+v"(qc[1][1]), "+v"(qc[1][2]), "+v"(qc[1][3]), \
                                   "+v"(qc[2][0]), "+v"(qc[2][1]), "+v"(qc[2][2]), "+v"(qc[2][3]), "+v"(qc[3][0]), "+v"(qc[3][1]), "+v"(qc[3][2]), "+v"(qc[3][3]))
    BAR_C();
    ST(1);
    epv = __hip_atomic_load(epoch_p, __ATOMIC_RELAXED, __HIP_MEMORY_SCOPE_AGENT);
    rd(ga0, gb0, 0, 0);
    __builtin_amdgcn_sched_barrier(0);
#pragma unroll
    for (int t = 0; t < 7; ++t) {
      rd(ga1, gb1, t % 3, 1);
      mm16(ga0, gb0, t == 0);
      SG_HALF16();
      PIN_QC();
      BAR_C();
      if (t < 6) {
        rd(ga0, gb0, (t + 1) % 3, 0);
        mm16(ga1, gb1, false);
        SG_HALF16(); PIN_QC(); __builtin_amdgcn_sched_barrier(0);
      }
    }
#pragma unroll
    for (int i = 0; i <= 4; ++i) {
      if (i < 4) {
#pragma unroll
        for (int j = 0; j < 4; ++j) qc[i][j] = MFMA(ga1[i], gb1[j], qc[i][j]);
        asm volatile("" : "+v"(qc[i][0]), "+v"(qc[i][1]), "+v"(qc[i][2]), "+v"(qc[i][3]));
      }
      if (i > 0) {
#pragma unroll
        for (int j = 0; j < 4; ++j)
          *(LAS u32x2*)(size_t)(qa + j * 8192 + (i - 1) * 512) = u32x2{pk(qc[i - 1][j][0], qc[i - 1][j][1]), pk(qc[i - 1][j][2], qc[i - 1][j][3])};
        if (i < 4) { SG_MFMA(1); SG_VALU(2); SG_MFMA(1); SG_VALU(2); SG_DSW(1); SG_MFMA(1); SG_VALU(2); SG_DSW(1); SG_MFMA(1); SG_VALU(2); SG_DSW(2); }
      }
      __builtin_amdgcn_sched_barrier(0);
    }
  }
  ST(2);
  BAR_C();
  ST(3);
  const unsigned want = next_epoch(__builtin_amdgcn_readfirstlane(epv));
  u32x4 pv[2];
  {
    f32x4 sc[4];
    bf16x8 qf[8], kf[8][4];
    auto rdS = [&](int s) {
      qf[s] = ld128(own + s * 1024);
#pragma unroll
      for (int kt = 0; kt < 4; ++kt) kf[s][kt] = ld128(kad[s & 3] + kt * 8192 + (s >> 2) * 256);
      __builtin_amdgcn_sched_barrier(0);
    };
    rdS(0); rdS(1); rdS(2);
#pragma unroll
    for (int s = 0; s < 8; ++s) {
#pragma unroll
      for (int kt = 0; kt < 4; ++kt) sc[kt] = MFMA(kf[s][kt], qf[s], s == 0 ? ZERO4 : sc[kt]);
      asm volatile("" : "+v"(sc[0]), "+v"(sc[1]), "+v"(sc[2]), "+v"(sc[3]));
      __builtin_amdgcn_sched_barrier(0);
      if (s < 5) rdS(s + 3);
    }
    ST(4);
    float mx = max3(max3(sc[0][0], sc[0][1], sc[0][2]), max3(sc[0][3], sc[1][0], sc[1][1]), max3(sc[1][2], sc[1][3], sc[2][0]));
    mx = max3(mx, max3(sc[2][1], sc[2][2], sc[2][3]), max3(max2(sc[3][0], sc[3][1]), sc[3][2], sc[3][3]));
    mx = xmax(mx);
    constexpr float C = 0.0625f * 1.4426950408889634f;
    const float mc = mx * C;
    float l = 0.f;
#pragma unroll
    for (int kt = 0; kt < 4; ++kt)
#pragma unroll
      for (int r = 0; r < 4; ++r) { sc[kt][r] = __builtin_amdgcn_exp2f(fmaf(sc[kt][r], C, -mc)); l += sc[kt][r]; }
    l = xsum(l);
    const float inv = __builtin_amdgcn_rcpf(l);
#pragma unroll
    for (int e = 0; e < 2; ++e) {
      pv[e] = __builtin_bit_cast(u32x4, pk8(sc[2 * e] * inv, sc[2 * e + 1] * inv));
      __builtin_amdgcn_raw_buffer_store_b128(pv[e], rs, pgo + e * 1024, 0, 16);
    }
  }
  ST(5);
  if (m == 7) {
    asm volatile("s_waitcnt vmcnt(0)" ::: "memory");
    if (lane == 0) __hip_atomic_store(pflag_p + m * 4 + w, want, __ATOMIC_RELAXED, __HIP_MEMORY_SCOPE_AGENT);
    STOUT();
    return;
  }
#define FENCE() __builtin_amdgcn_sched_barrier(0)
  f32x4 o[2][4];
  {
    f32x4 acc[4][4];
    bf16x8 fa0[4], fb0[4], fa1[4], fb1[4];
    auto rd = [&](bf16x8 (&fa)[4], bf16x8 (&fb)[4], int slot, int half) {
#pragma unroll
      for (int i = 0; i < 4; ++i) {
        const unsigned lo_ = (slot & 1) * 32768;
        fa[i] = ld128(vba[slot >> 1][half] + lo_ + i * 2048);
        const unsigned wa_ = wba[slot >> 1][i & 1] + lo_ + (i >> 1) * 8192 + half * 4096;
        fb[i] = ldtr(wa_, wa_ + 512);
      }
    };
    auto mm = [&](const bf16x8 (&fa)[4], const bf16x8 (&fb)[4], bool first) {
#pragma unroll
      for (int j = 0; j < 4; ++j)
#pragma unroll
        for (int i = 0; i < 4; ++i) acc[j][i] = MFMA(fa[i], fb[j], first ? ZERO4 : acc[j][i]);
    };
#define PIN_ACC() asm volatile("" : "+v"(acc[0][0]), "+v"(acc[0][1]), "+v"(acc[0][2]), "+v"(acc[0][3]), "+v"(acc[1][0]), "+v"(acc[1][1]), "+v"(acc[1][2]), "+v"(acc[1][3]), \
                                    "+v"(acc[2][0]), "+v"(acc[2][1]), "+v"(acc[2][2]), "+v"(acc[2][3]), "+v"(acc[3][0]), "+v"(acc[3][1]), "+v"(acc[3][2]), "+v"(acc[3][3]))
    bf16x8 cf0[4][2];
    BAR_C();
    ST(6);
    rd(fa0, fb0, zslot(0), 0);
    *(LAS u32x4*)(size_t)plo = pv[0];
    *(LAS u32x4*)(size_t)(plo + 1024) = pv[1];
    FENCE();
#pragma unroll
    for (int z = 0; z < 8; ++z) {
      rd(fa1, fb1, zslot(z), 1);
      mm(fa0, fb0, (z & 3) == 0);
      if (z == 4) asm volatile("" : "+v"(cf0[0][0]), "+v"(cf0[0][1]), "+v"(cf0[1][0]), "+v"(cf0[1][1]), "+v"(cf0[2][0]), "+v"(cf0[2][1]), "+v"(cf0[3][0]), "+v"(cf0[3][1]));
      SG_HALF16();
      PIN_ACC();
      if (z == PFLAG_AT) {
        asm volatile("s_waitcnt vmcnt(0)" ::: "memory");
        if (lane == 0) __hip_atomic_store(pflag_p + m * 4 + w, want, __ATOMIC_RELAXED, __HIP_MEMORY_SCOPE_AGENT);
      }
      BAR_C();
      if (z == 4) ST(8);
      if (z == 5) ST(9);
      if (z == 6) ST(10);
      if (z < 7) {
        rd(fa0, fb0, zslot(z + 1), 0);
        mm(fa1, fb1, false);
        if (z == 3) {
#pragma unroll
          for (int j = 0; j < 4; ++j) { cf0[j][0] = pk8(acc[j][0], acc[j][1]); cf0[j][1] = pk8(acc[j][2], acc[j][3]); }
          SG_MFMA(1); SG_DSR(2); SG_MFMA(1); SG_DSR(2);
#pragma unroll
          for (int i = 0; i < 8; ++i) { SG_MFMA(1); SG_DSR(1); }
#pragma unroll
          for (int i = 0; i < 6; ++i) { SG_MFMA(1); SG_VALU(2); }
          SG_VALU(20);
        } else {
          SG_HALF16();
          PIN_ACC();
        }
        FENCE();
      }
    }
    {
      bf16x8 pf[2][4][2][2], cf[4][2];
      auto rdP = [&](int G, int qt) {
#pragma unroll
        for (int i = 0; i < 4; ++i) pf[G][qt][i >> 1][i & 1] = ld128((G ? pad2[qt] : pad[qt]) + (i >> 1) * 8192 + (i & 1) * 1024);
      };
      auto pc = [&](int G, int qt) {
#pragma unroll
        for (int i = 0; i < 4; ++i)
#pragma unroll
          for (int nt = 0; nt < 2; ++nt)
            o[nt][qt] = MFMA((G ? cf : cf0)[(i & 2) + nt][i & 1], pf[G][qt][i >> 1][i & 1], (G == 0 && i == 0) ? ZERO4 : o[nt][qt]);
      };
      auto pin = [&](int qt) { asm volatile("" : "+v"(o[0][qt]), "+v"(o[1][qt])); FENCE(); };
      auto send = [&](int qt) {
        __builtin_amdgcn_raw_buffer_store_b128(__builtin_bit_cast(u32x4, pk8(o[0][qt], o[1][qt])), rs, fo + ((gp ^ qt) << 14), 0, 16);
        FENCE();
      };
#define SG_PC(nr, nv) do { _Pragma("unroll") for (int i_ = 0; i_ < 4; ++i_) { SG_MFMA(1); if (nr) SG_DSR(1); SG_MFMA(1); if (nv) SG_VALU(nv); } } while (0)
#pragma unroll
      for (int j = 0; j < 4; ++j) {
#pragma unroll
        for (int i = 0; i < 4; ++i) acc[j][i] = MFMA(fa1[i], fb1[j], acc[j][i]);
        if (j == 0) rdP(0, 3);
        if (j == 1) rdP(0, 2);
        if (j == 2) rdP(1, 3);
        if (j == 3) rdP(1, 2);
#pragma unroll
        for (int i = 0; i < 4; ++i) { SG_MFMA(1); SG_DSR(1); }
        asm volatile("" : "+v"(acc[j][0]), "+v"(acc[j][1]), "+v"(acc[j][2]), "+v"(acc[j][3]));
        FENCE();
      }
      pc(0, 3); rdP(0, 1);
      cf[0][0] = pk8(acc[0][0], acc[0][1]); cf[0][1] = pk8(acc[0][2], acc[0][3]); cf[1][0] = pk8(acc[1][0], acc[1][1]); cf[1][1] = pk8(acc[1][2], acc[1][3]);
      SG_PC(1, 4);
      asm volatile("" : "+v"(cf[0][0]), "+v"(cf[0][1]), "+v"(cf[1][0]), "+v"(cf[1][1])); pin(3);
      pc(0, 2); rdP(1, 1);
      cf[2][0] = pk8(acc[2][0], acc[2][1]); cf[2][1] = pk8(acc[2][2], acc[2][3]); cf[3][0] = pk8(acc[3][0], acc[3][1]); cf[3][1] = pk8(acc[3][2], acc[3][3]);
      SG_PC(1, 4);
      asm volatile("" : "+v"(cf[2][0]), "+v"(cf[2][1]), "+v"(cf[3][0]), "+v"(cf[3][1])); pin(2);
      pc(1, 3); rdP(0, 0); SG_PC(1, 0); pin(3);
      pc(1, 2); rdP(1, 0); SG_PC(1, 0); pin(2);
      send(3);
      pc(0, 1); pin(1);
      send(2);
      pc(1, 1); pin(1);
      pc(0, 0); pin(0);
      send(1);
      pc(1, 0); pin(0);
    }
  }
  ST(11);
  asm volatile("s_waitcnt vmcnt(0)" ::: "memory");
  if (lane < 4 && lane != gp) __hip_atomic_store(fs, want, __ATOMIC_RELAXED, __HIP_MEMORY_SCOPE_AGENT);
  ST(12);
  {
    u32x4 d[3];
    for (unsigned spins = 0; spins < SPIN_CAP; ++spins) {
      const unsigned fl = __hip_atomic_load(fp, __ATOMIC_RELAXED, __HIP_MEMORY_SCOPE_AGENT);
      if (__all(fl == want || (lane & 3) == gp)) break;
      __builtin_amdgcn_s_sleep(POLL_SLEEP);
    }
    asm volatile("" ::: "memory");
    ST(13);
#pragma unroll
    for (int k = 0; k < 3; ++k) d[k] = __builtin_amdgcn_raw_buffer_load_b128(rs, fi, (gp ^ (k + 1)) * 4096, 16);
    float a[8];
#pragma unroll
    for (int i = 0; i < 4; ++i) { a[i] = o[0][0][i]; a[4 + i] = o[1][0][i]; }
#pragma unroll
    for (int k = 0; k < 3; ++k)
#pragma unroll
      for (int i = 0; i < 4; ++i) { a[2 * i] += __uint_as_float(d[k][i] << 16); a[2 * i + 1] += __uint_as_float(d[k][i] & 0xffff0000u); }
    const auto s0 = __builtin_amdgcn_permlane16_swap(pk(a[0], a[1]), pk(a[4], a[5]), false, false);
    const auto s1 = __builtin_amdgcn_permlane16_swap(pk(a[2], a[3]), pk(a[6], a[7]), false, false);
    *(u32x4*)op = u32x4{s0[0], s1[0], s0[1], s1[1]};
    if (gp == 2 && m == 0 && w == 0 && lane == 0) __hip_atomic_store(epoch_p, want, __ATOMIC_RELAXED, __HIP_MEMORY_SCOPE_AGENT);
  }
  ST(14);
  STOUT();
}

extern "C" void kernel_launch(void* const* d_in, const int* in_sizes, int n_in, void* d_out, int out_size,
                              void* d_ws, size_t ws_size, hipStream_t stream) {
  Params p;
  p.x0 = (const char*)d_in[0]; p.k0 = (const char*)d_in[1]; p.v0 = (const char*)d_in[2]; p.wq0 = (const char*)d_in[3]; p.wo0 = (const char*)d_in[4];
  p.x1 = (const char*)d_in[5]; p.k1 = (const char*)d_in[6]; p.v1 = (const char*)d_in[7]; p.wq1 = (const char*)d_in[8]; p.wo1 = (const char*)d_in[9];
  p.out = (char*)d_out; p.ws = (char*)d_ws;
  gqa_fused<<<256, 512, 0, stream>>>(p);
}
```

```cpp
#include <hip/hip_runtime.h>
#include <hip/hip_bf16.h>

typedef short s16x4 __attribute__((ext_vector_type(4)));
typedef short bf16x8 __attribute__((ext_vector_type(8)));
typedef float f32x4 __attribute__((ext_vector_type(4)));
typedef float f32x2 __attribute__((ext_vector_type(2)));
typedef unsigned u32x4 __attribute__((ext_vector_type(4)));
typedef unsigned u32x2 __attribute__((ext_vector_type(2)));
typedef unsigned u32x16 __attribute__((ext_vector_type(16)));
typedef unsigned u32x8 __attribute__((ext_vector_type(8)));
typedef __bf16 bf16x2_t __attribute__((ext_vector_type(2)));
#define LAS __attribute__((address_space(3)))
#define GAS __attribute__((address_space(1)))
#define DI __device__ __forceinline__

struct Params {
  const char* x0; const char* k0; const char* v0; const char* wq0; const char* wo0;
  const char* x1; const char* k1; const char* v1; const char* wq1; const char* wo1;
  char* out; char* ws;
};

constexpr unsigned WS_EPOCH = 0;
constexpr unsigned WS_PFLAG = 4096;
constexpr unsigned WS_FFLAG = 65536;
constexpr unsigned WS_P = 131072;
constexpr unsigned WS_F = WS_P + 32 * 65536;
constexpr unsigned WS_END = WS_F + 64 * 65536;

constexpr unsigned SW = 0;
constexpr unsigned SX = 131072;

DI unsigned pk(float a, float b) { f32x2 v = {a, b}; return __builtin_bit_cast(unsigned, __builtin_convertvector(v, bf16x2_t)); }
DI bf16x8 pk8(f32x4 a, f32x4 b) { u32x4 r = {pk(a[0], a[1]), pk(a[2], a[3]), pk(b[0], b[1]), pk(b[2], b[3])}; return __builtin_bit_cast(bf16x8, r); }
DI bf16x8 ld128(unsigned a) { return *(LAS const bf16x8*)(size_t)a; }
DI bf16x8 ldtr(unsigned a0, unsigned a1) {
  s16x4 lo = __builtin_amdgcn_ds_read_tr16_b64_v4i16((LAS s16x4*)(size_t)a0);
  s16x4 hi = __builtin_amdgcn_ds_read_tr16_b64_v4i16((LAS s16x4*)(size_t)a1);
  return __builtin_shufflevector(lo, hi, 0, 1, 2, 3, 4, 5, 6, 7);
}
#define MFMA(a, b, c) __builtin_amdgcn_mfma_f32_16x16x32_bf16((a), (b), (c), 0, 0, 0)
#define ZERO4 (f32x4{0.f, 0.f, 0.f, 0.f})
#define BAR_C() do { __builtin_amdgcn_sched_barrier(0); asm volatile("" ::: "memory"); __builtin_amdgcn_s_waitcnt(0xC07F); \
                     __builtin_amdgcn_s_barrier(); asm volatile("" ::: "memory"); __builtin_amdgcn_sched_barrier(0); } while (0)
#define BAR_L(n) asm volatile("s_waitcnt vmcnt(" #n ")\n\ts_barrier" ::: "memory")
#define SG_MFMA(n) __builtin_amdgcn_sched_group_barrier(0x008, (n), 0)
#define SG_DSR(n) __builtin_amdgcn_sched_group_barrier(0x100, (n), 0)
#define SG_DSW(n) __builtin_amdgcn_sched_group_barrier(0x200, (n), 0)
#define SG_VALU(n) __builtin_amdgcn_sched_group_barrier(0x002, (n), 0)
constexpr int THR = 11;
#define GL(n, pol) "s_waitcnt vmcnt(%[thr])\n\tglobal_load_lds_dwordx4 %" #n ", %[sb]" pol "\n\ts_add_u32 m0, m0, 0x1000\n\t"
DI void glds2(const char* sb, unsigned lds_addr, unsigned v0, unsigned v1) {
  asm volatile("s_mov_b32 m0, %[la]\n\t" GL(0, "") GL(1, "") :: "v"(v0), "v"(v1), [sb] "s"(sb), [la] "s"(lds_addr), [thr] "i"(THR) : "memory", "scc");
}
DI void glds4(const char* sb, unsigned lds_addr, const unsigned (&v)[4]) {
  asm volatile("s_mov_b32 m0, %[la]\n\t" GL(0, "") GL(1, "") GL(2, "") GL(3, "")
               :: "v"(v[0]), "v"(v[1]), "v"(v[2]), "v"(v[3]), [sb] "s"(sb), [la] "s"(lds_addr), [thr] "i"(THR) : "memory", "scc");
}
DI void glds8(const char* sb, unsigned lds_addr, const unsigned (&v)[8]) {
  asm volatile("s_mov_b32 m0, %[la]\n\t" GL(0, "") GL(1, "") GL(2, "") GL(3, "") GL(4, "") GL(5, "") GL(6, "") GL(7, "")
               :: "v"(v[0]), "v"(v[1]), "v"(v[2]), "v"(v[3]), "v"(v[4]), "v"(v[5]), "v"(v[6]), "v"(v[7]),
                  [sb] "s"(sb), [la] "s"(lds_addr), [thr] "i"(THR) : "memory", "scc");
}
DI void glds2_dev(const char* sb, unsigned lds_addr, unsigned v0, unsigned v1) {
  asm volatile("s_mov_b32 m0, %[la]\n\t" GL(0, " sc1") GL(1, " sc1") :: "v"(v0), "v"(v1), [sb] "s"(sb), [la] "s"(lds_addr), [thr] "i"(THR) : "memory", "scc");
}
DI unsigned next_epoch(unsigned e) { return e * 0x9E3779B1u + 0x7F4A7C15u; }
DI float max2(float a, float b) { return __builtin_elementwise_maximum(a, b); }
DI float max3(float a, float b, float c) { return max2(max2(a, b), c); }
DI float xmax(float v) {
  auto a = __builtin_amdgcn_permlane32_swap(__float_as_uint(v), __float_as_uint(v), false, false);
  v = max2(__uint_as_float(a[0]), __uint_as_float(a[1]));
  auto b = __builtin_amdgcn_permlane16_swap(__float_as_uint(v), __float_as_uint(v), false, false);
  return max2(__uint_as_float(b[0]), __uint_as_float(b[1]));
}
DI float xsum(float v) {
  auto a = __builtin_amdgcn_permlane32_swap(__float_as_uint(v), __float_as_uint(v), false, false);
  v = __uint_as_float(a[0]) + __uint_as_float(a[1]);
  auto b = __builtin_amdgcn_permlane16_swap(__float_as_uint(v), __float_as_uint(v), false, false);
  return __uint_as_float(b[0]) + __uint_as_float(b[1]);
}
constexpr int zslot(int z) { return (0x30213021 >> (4 * z)) & 3; }
constexpr int CODE_BYTES = 21120;
constexpr unsigned SPIN_CAP = 1u << 18;

__global__ __launch_bounds__(512, 2) void gqa_fused(Params P) {
  __shared__ __attribute__((aligned(1024))) char lds[163840];
  u32x16 ka; u32x8 kb8;
  asm volatile("s_load_dwordx16 %0, %2, 0x0\n\ts_load_dwordx8 %1, %2, 0x40" : "=&s"(ka), "=&s"(kb8) : "s"(__builtin_amdgcn_kernarg_segment_ptr()));
  const int tid = threadIdx.x, lane = tid & 63;
  const int w = __builtin_amdgcn_readfirstlane(tid >> 6);
  const int bid = blockIdx.x;
  const int xcd = bid & 7, sl = bid >> 3;
  const int br = xcd >> 2, gp = xcd & 3;
  const int bp = sl >> 3, m = sl & 7;
  const int hj = m & 3, h = gp * 4 + hj, b = bp * 2 + (m >> 2);
  const int team = xcd * 4 + bp, comp = br * 4 + bp, grp = comp * 8 + m;
  const unsigned lds0 = (unsigned)(size_t)(LAS char*)lds;
  const int k1 = (bp + m) & 3;
  {
    unsigned long long pc; asm volatile("s_getpc_b64 %0" : "=s"(pc));
    const unsigned vo = min((sl * 8 + w) * 128, CODE_BYTES - 128);
    asm volatile("s_mov_b32 m0, %2\n\ts_nop 0\n\tglobal_load_lds_dword %0, %1" :: "v"(vo), "s"(pc & ~127ull), "s"(lds0 + SX + 3 * 8192 + w * 256) : "memory");
  }
  const int lw = w & 3;
  const unsigned hi = lane >> 5, l31 = lane & 31;
  unsigned v_x = (lane >> 3) * 896 + (((lane & 7) ^ (4 * (lw & 1) + (lane >> 4))) * 16);
  unsigned v_x1 = v_x + 32 * 896;
  unsigned v_wq[8];
#pragma unroll
  for (int i = 0; i < 8; ++i)
    v_wq[i] = hi * 8192 + i * (8 * 8192) + (l31 ^ (((2 * (lw & 1) + hi) | ((i & 1) << 2)) << 1)) * 16;
  int sa = (b >> 1) * 2 + 2 * hj;
  sa = sa >= 7 ? sa - 7 : sa;
  int ox = b * 57344 + lw * (8 * 896), owq = h * 512 + lw * (2 * 8192);
  asm volatile("" : "+v"(v_x), "+v"(v_x1), "+v"(v_wq[0]), "+v"(v_wq[1]), "+v"(v_wq[2]), "+v"(v_wq[3]), "+v"(v_wq[4]), "+v"(v_wq[5]), "+v"(v_wq[6]), "+v"(v_wq[7]));
  asm volatile("s_waitcnt lgkmcnt(0)" : "+s"(ka), "+s"(kb8), "+s"(sa), "+s"(ox), "+s"(owq));
#define KPTR(lo, hi) ((char*)(((unsigned long long)(hi) << 32) | (lo)))
#define K_X (br ? KPTR(ka[10], ka[11]) : KPTR(ka[0], ka[1]))
#define K_K (br ? KPTR(ka[12], ka[13]) : KPTR(ka[2], ka[3]))
#define K_V (br ? KPTR(ka[14], ka[15]) : KPTR(ka[4], ka[5]))
#define K_WQ (br ? KPTR(kb8[0], kb8[1]) : KPTR(ka[6], ka[7]))
#define K_WO (br ? KPTR(kb8[2], kb8[3]) : KPTR(ka[8], ka[9]))
#define K_OUT KPTR(kb8[4], kb8[5])
#define K_WS KPTR(kb8[6], kb8[7])

  if (__builtin_expect(w >= 4, 1)) {
    __builtin_amdgcn_s_setprio(3);
    const char* const xb = K_X + ox;
    const char* const wqb = K_WQ + owq;
    auto stageA = [&](int slot) {
      glds2(xb + sa * 128, lds0 + SX + slot * 8192 + lw * 1024, v_x, v_x1);
      glds8(wqb + sa * (64 * 8192), lds0 + SW + slot * 32768 + lw * 1024, v_wq);
      sa = sa == 6 ? 0 : sa + 1;
    };
    stageA(0);
    char* const ws = K_WS;
    GAS unsigned* const epoch_p = (GAS unsigned*)(ws + WS_EPOCH + comp * 256);
    GAS unsigned* const pflag_p = (GAS unsigned*)(ws + WS_PFLAG + team * 256);
    unsigned epv;
    unsigned v_k[4], v_wo[4], v_v[4];
#pragma unroll
    for (int i = 0; i < 4; ++i) {
      v_k[i] = ((hi * 512 + ((l31 ^ (2 * lw + hi)) * 16)) ^ ((i & 1) * 128)) + i * 4096;
      v_wo[i] = (lane >> 3) * 896 + (((lane & 7) ^ ((((lane >> 4) & 1) | ((lw & 1) << 1)) << 1)) * 16) + (i >> 1) * 229376 + (i & 1) * (32 * 896);
      v_v[i] = (lane >> 3) * 512 + (((lane & 7) ^ (4 * (lw & 1) + (lane >> 4))) * 16) + (i >> 1) * (8 * 32768) + (i & 1) * 16384;
    }
    const char* const kb = K_K + (b * 8 + (h >> 1)) * 32768 + lw * 1024;
    const char* const vb = K_V + (bp * 16 + gp * 2) * 32768 + lw * 4096;
    const char* const wob = K_WO + gp * (4 * 229376) + m * 128 + lw * (8 * 896);
    auto stageK = [&]() {
      const int f = (h & 1) * 16384;
      glds4(kb + f, lds0 + SW + 3 * 32768 + lw * 1024 + f, v_k);
      glds4(kb + (f ^ 16384), lds0 + SW + 3 * 32768 + lw * 1024 + (f ^ 16384), v_k);
    };
    auto stageZ = [&](int z, int part) {
      const int gl = z >> 2, kq = (k1 + z) & 3;
      const unsigned d = lds0 + SW + zslot(z) * 32768 + lw * 1024;
      if (part & 1) glds4(wob + gl * (2 * 229376) + kq * (64 * 896), d, v_wo);
      if (part & 2) glds4(vb + gl * 32768 + kq * 128, d + 16384, v_v);
    };
    stageA(1);
    BAR_L(10);
    asm volatile("global_load_dword %0, %1, %2 sc1" : "=v"(epv) : "v"(0), "s"(epoch_p) : "memory");
    stageA(2);
#pragma unroll 1
    for (int t = 0; t < 4; ++t) {
      BAR_L(10);
      stageA(t == 3 ? 0 : t);
      if (m == 7 && t == 0) {
        const char* vt = vb + lw * 4096;
        asm volatile("s_mov_b32 m0, %3\n\ts_nop 0\n\tglobal_load_lds_dword %0, %1\n\tglobal_load_lds_dword %0, %2"
                     :: "v"(lane * 128), "s"(vt), "s"(vt + 8 * 32768), "s"(lds0 + SX + 3 * 8192 + 2048 + lw * 256) : "memory");
      }
    }
    asm volatile("" : "+v"(epv));
    const unsigned ep = __builtin_amdgcn_readfirstlane(epv);
    BAR_L(10); stageK();
    if (m == 7) {
      BAR_L(8); BAR_L(63); BAR_L(0);
      return;
    }
    BAR_L(8); stageZ(0, 1);
    BAR_L(63); stageZ(0, 2);
    BAR_L(8); stageZ(1, 3); stageZ(2, 1);
    BAR_L(12); stageZ(2, 2); stageZ(3, 1);
    BAR_L(12); stageZ(3, 2); stageZ(4, 1);
    BAR_L(12); stageZ(4, 2); stageZ(5, 1);
    unsigned fl;
    const unsigned voff_f = (lane & 31) * 4;
    const unsigned want = next_epoch(ep);
    auto stageP = [&](int k0, int k1_) {
      const char* src = ws + WS_P + team * 65536 + lw * 1024;
      const unsigned v_lin = lane * 16, v_lin1 = v_lin + 4096;
#pragma unroll
      for (int k = k0; k < k1_; ++k) {
        int mm = m + k; mm = k == 7 ? 7 : mm >= 7 ? mm - 7 : mm;
        glds2_dev(src + mm * 8192, lds0 + (((mm ^ m) & 4) ? SW + 1 * 32768 : SX) + (mm & 3) * 8192 + lw * 1024, v_lin, v_lin1);
      }
    };
#define LOAD_FLAGS() asm volatile("global_load_dword %0, %1, %2 sc1" : "=v"(fl) : "v"(voff_f), "s"(pflag_p) : "memory")
#define SPIN_FLAGS(ok) for (unsigned spins = 0; !__all(ok) && spins < SPIN_CAP; ++spins) \
      asm volatile("s_sleep 1\n\tglobal_load_dword %0, %1, %2 sc1\n\ts_waitcnt vmcnt(0)" : "=v"(fl) : "v"(voff_f), "s"(pflag_p) : "memory")
    BAR_L(12); stageZ(5, 2); stageZ(6, 1);
    BAR_L(12); LOAD_FLAGS();
    stageZ(6, 2); stageZ(7, 1);
    BAR_L(13); stageZ(7, 2);
    asm volatile("s_waitcnt vmcnt(12)" : "+v"(fl) :: "memory");
    SPIN_FLAGS(fl == want || (lane & 31) >= 28);
    stageP(1, 3);
    BAR_L(12); LOAD_FLAGS();
    stageP(3, 7);
    BAR_L(12);
    asm volatile("s_waitcnt vmcnt(8)" : "+v"(fl) :: "memory");
    SPIN_FLAGS(fl == want);
    stageP(7, 8);
    BAR_L(0);
    return;
  }

  const unsigned r16 = lane & 15, g = lane >> 4, q4 = r16 >> 2, p4 = lane & 3;
  char* const ws = K_WS;
  GAS unsigned* const epoch_p = (GAS unsigned*)(ws + WS_EPOCH + comp * 256);
  GAS unsigned* const pflag_p = (GAS unsigned*)(ws + WS_PFLAG + team * 256);
  asm volatile("s_waitcnt vmcnt(0)" ::: "memory");
  const __amdgpu_buffer_rsrc_t rs = __builtin_amdgcn_make_buffer_rsrc(ws, 0, WS_END, 0x00020000);
  const int ct = w & 1, bi = w >> 1;
  unsigned qa = lds0 + SX + w * 2048 + (g >> 1) * 256 + r16 * 16 + (g & 1) * 8;
  unsigned own = lds0 + SX + w * 8192 + lane * 16;
  unsigned kad[4];
#pragma unroll
  for (int i = 0; i < 4; ++i) kad[i] = (lds0 + SW + 3 * 32768 + r16 * 512 + ((g ^ r16) * 16)) ^ (i * 64);
  const unsigned po = w * 2048 + lane * 16;
  unsigned pgo = WS_P + team * 65536 + m * 8192 + po;
  unsigned plo = lds0 + SX + (m & 3) * 8192 + po;
  unsigned wba[2][2], vba[2][2], pad[4];
#pragma unroll
  for (int u = 0; u < 2; ++u)
#pragma unroll
    for (int i = 0; i < 2; ++i) {
      wba[u][i] = lds0 + SW + u * 65536 + (8 * g + q4) * 128 + (((4 * ct + 2 * i + (p4 >> 1)) ^ (((q4 >> 1) | ((g & 1) << 1)) << 1)) * 16) + (p4 & 1) * 8;
      vba[u][i] = lds0 + SW + u * 65536 + 16384 + bi * 8192 + r16 * 128 + (((4 * i + g) ^ ((r16 >> 1) & 7)) * 16);
    }
#pragma unroll
  for (int i = 0; i < 4; ++i) pad[i] = lds0 + (bi != (m >> 2) ? SW + 1 * 32768 : SX) + (gp ^ i) * 2048 + lane * 16;
  asm volatile("" : "+v"(wba[0][0]), "+v"(wba[0][1]), "+v"(wba[1][0]), "+v"(wba[1][1]), "+v"(vba[0][0]), "+v"(vba[0][1]), "+v"(vba[1][0]), "+v"(vba[1][1]),
                    "+v"(pad[0]), "+v"(pad[1]), "+v"(pad[2]), "+v"(pad[3]));
  unsigned fo = WS_F + grp * 65536 + gp * 4096 + w * 1024 + lane * 16;
  unsigned fi = WS_F + grp * 65536 + gp * 16384 + w * 1024 + lane * 16;
  GAS unsigned* const fflag_p = (GAS unsigned*)(ws + WS_FFLAG + grp * 1024 + w * 64);
  GAS unsigned* fp = fflag_p + gp * 64 + (lane & 3);
  GAS unsigned* fs = fflag_p + (lane & 3) * 64 + gp;
  GAS u32x4* op = (GAS u32x4*)(K_OUT + (((bp * 2 + bi) * 64 + gp * 16 + r16) * 896 + br * 448 + m * 64 + ct * 32 + (g & 1) * 16 + (g >> 1) * 8) * 2);
  asm volatile("" : "+v"(fo), "+v"(fi), "+v"(op), "+v"(fp), "+v"(fs));
  asm volatile("" : "+v"(qa), "+v"(own), "+v"(kad[0]), "+v"(kad[1]), "+v"(kad[2]), "+v"(kad[3]), "+v"(pgo), "+v"(plo));
  unsigned epv;
  {
    f32x4 qc[4][4];
    bf16x8 ga0[4], gb0[4], ga1[4], gb1[4];
    const unsigned xa = lds0 + SX + r16 * 128 + ((g ^ ((r16 >> 1) & 7)) * 16);
    const unsigned wa = lds0 + SW + (8 * g + q4) * 512 + (((8 * w + (p4 >> 1)) ^ ((q4 | ((g & 1) << 2)) << 1)) * 16) + (p4 & 1) * 8;
    unsigned xad[2] = {xa, xa ^ 64}, wad[2][4];
#pragma unroll
    for (int i = 0; i < 4; ++i) { wad[0][i] = wa ^ (i * 32); wad[1][i] = wad[0][i] + 65536; }
    asm volatile("" : "+v"(xad[0]), "+v"(xad[1]), "+v"(wad[0][0]), "+v"(wad[0][1]), "+v"(wad[0][2]), "+v"(wad[0][3]),
                      "+v"(wad[1][0]), "+v"(wad[1][1]), "+v"(wad[1][2]), "+v"(wad[1][3]));
    auto rd = [&](bf16x8 (&fa)[4], bf16x8 (&fb)[4], int slot, int half) {
#pragma unroll
      for (int i = 0; i < 4; ++i) {
        const unsigned wo_ = (slot & 1) * 32768 + half * 16384;
        fb[i] = ld128(xad[half] + slot * 8192 + i * 2048);
        fa[i] = ldtr(wad[slot >> 1][i] + wo_, wad[slot >> 1][i] + wo_ + 2048);
      }
    };
    auto mm16 = [&](const bf16x8 (&fa)[4], const bf16x8 (&fb)[4], bool first) {
#pragma unroll
      for (int i = 0; i < 4; ++i)
#pragma unroll
        for (int j = 0; j < 4; ++j) qc[i][j] = MFMA(fa[i], fb[j], first ? ZERO4 : qc[i][j]);
    };
#define SG_HALF16() do { SG_MFMA(1); SG_DSR(2); SG_MFMA(1); SG_DSR(2); _Pragma("unroll") for (int i_ = 0; i_ < 8; ++i_) { SG_MFMA(1); SG_DSR(1); } SG_MFMA(6); } while (0)
#define PIN_QC() asm volatile("" : "+v"(qc[0][0]), "+v"(qc[0][1]), "+v"(qc[0][2]), "+v"(qc[0][3]), "+v"(qc[1][0]), "+v"(qc[1][1]), "+v"(qc[1][2]), "+v"(qc[1][3]), \
                                   "+v"(qc[2][0]), "+v"(qc[2][1]), "+v"(qc[2][2]), "+v"(qc[2][3]), "+v"(qc[3][0]), "+v"(qc[3][1]), "+v"(qc[3][2]), "+v"(qc[3][3]))
    BAR_C();
    epv = __hip_atomic_load(epoch_p, __ATOMIC_RELAXED, __HIP_MEMORY_SCOPE_AGENT);
    rd(ga0, gb0, 0, 0);
    __builtin_amdgcn_sched_barrier(0);
#pragma unroll
    for (int t = 0; t < 7; ++t) {
      rd(ga1, gb1, t % 3, 1);
      mm16(ga0, gb0, t == 0);
      SG_HALF16();
      PIN_QC();
      BAR_C();
      if (t < 6) {
        rd(ga0, gb0, (t + 1) % 3, 0);
        mm16(ga1, gb1, false);
        SG_HALF16(); PIN_QC(); __builtin_amdgcn_sched_barrier(0);
      }
    }
#pragma unroll
    for (int i = 0; i <= 4; ++i) {
      if (i < 4) {
#pragma unroll
        for (int j = 0; j < 4; ++j) qc[i][j] = MFMA(ga1[i], gb1[j], qc[i][j]);
        asm volatile("" : "+v"(qc[i][0]), "+v"(qc[i][1]), "+v"(qc[i][2]), "+v"(qc[i][3]));
      }
      if (i > 0) {
#pragma unroll
        for (int j = 0; j < 4; ++j)
          *(LAS u32x2*)(size_t)(qa + j * 8192 + (i - 1) * 512) = u32x2{pk(qc[i - 1][j][0], qc[i - 1][j][1]), pk(qc[i - 1][j][2], qc[i - 1][j][3])};
        if (i < 4) { SG_MFMA(1); SG_VALU(2); SG_MFMA(1); SG_VALU(2); SG_DSW(1); SG_MFMA(1); SG_VALU(2); SG_DSW(1); SG_MFMA(1); SG_VALU(2); SG_DSW(2); }
      }
      __builtin_amdgcn_sched_barrier(0);
    }
  }
  BAR_C();
  const unsigned want = next_epoch(__builtin_amdgcn_readfirstlane(epv));
  u32x4 pv[2];
  {
    f32x4 sc[4];
    bf16x8 qf[8], kf[8][4];
    auto rdS = [&](int s) {
      qf[s] = ld128(own + s * 1024);
#pragma unroll
      for (int kt = 0; kt < 4; ++kt) kf[s][kt] = ld128(kad[s & 3] + kt * 8192 + (s >> 2) * 256);
      __builtin_amdgcn_sched_barrier(0);
    };
    rdS(0); rdS(1); rdS(2);
#pragma unroll
    for (int s = 0; s < 8; ++s) {
#pragma unroll
      for (int kt = 0; kt < 4; ++kt) sc[kt] = MFMA(kf[s][kt], qf[s], s == 0 ? ZERO4 : sc[kt]);
      asm volatile("" : "+v"(sc[0]), "+v"(sc[1]), "+v"(sc[2]), "+v"(sc[3]));
      __builtin_amdgcn_sched_barrier(0);
      if (s < 5) rdS(s + 3);
    }
    float mx = max3(max3(sc[0][0], sc[0][1], sc[0][2]), max3(sc[0][3], sc[1][0], sc[1][1]), max3(sc[1][2], sc[1][3], sc[2][0]));
    mx = max3(mx, max3(sc[2][1], sc[2][2], sc[2][3]), max3(max2(sc[3][0], sc[3][1]), sc[3][2], sc[3][3]));
    mx = xmax(mx);
    constexpr float C = 0.0625f * 1.4426950408889634f;
    const float mc = mx * C;
    float l = 0.f;
#pragma unroll
    for (int kt = 0; kt < 4; ++kt)
#pragma unroll
      for (int r = 0; r < 4; ++r) { sc[kt][r] = __builtin_amdgcn_exp2f(fmaf(sc[kt][r], C, -mc)); l += sc[kt][r]; }
    l = xsum(l);
    const float inv = __builtin_amdgcn_rcpf(l);
#pragma unroll
    for (int e = 0; e < 2; ++e) {
      pv[e] = __builtin_bit_cast(u32x4, pk8(sc[2 * e] * inv, sc[2 * e + 1] * inv));
      __builtin_amdgcn_raw_buffer_store_b128(pv[e], rs, pgo + e * 1024, 0, 16);
    }
  }
  if (m == 7) {
    asm volatile("s_waitcnt vmcnt(0)" ::: "memory");
    if (lane == 0) __hip_atomic_store(pflag_p + m * 4 + w, want, __ATOMIC_RELAXED, __HIP_MEMORY_SCOPE_AGENT);
    return;
  }
#define FENCE() __builtin_amdgcn_sched_barrier(0)
  f32x4 o[2][4];
  {
    f32x4 acc[4][4];
    bf16x8 fa0[4], fb0[4], fa1[4], fb1[4];
    auto rd = [&](bf16x8 (&fa)[4], bf16x8 (&fb)[4], int z, int half) {
      const int slot = zslot(z);
#pragma unroll
      for (int i = 0; i < 4; ++i) {
        const unsigned lo_ = (slot & 1) * 32768;
        fa[i] = ld128(vba[slot >> 1][half] + lo_ + i * 2048);
        const unsigned wa_ = wba[slot >> 1][i & 1] + lo_ + (i >> 1) * 8192 + half * 4096;
        fb[i] = ldtr(wa_, wa_ + 512);
      }
    };
    auto mm = [&](const bf16x8 (&fa)[4], const bf16x8 (&fb)[4], bool first) {
#pragma unroll
      for (int j = 0; j < 4; ++j)
#pragma unroll
        for (int i = 0; i < 4; ++i) acc[j][i] = MFMA(fa[i], fb[j], first ? ZERO4 : acc[j][i]);
    };
#define PIN_ACC() asm volatile("" : "+v"(acc[0][0]), "+v"(acc[0][1]), "+v"(acc[0][2]), "+v"(acc[0][3]), "+v"(acc[1][0]), "+v"(acc[1][1]), "+v"(acc[1][2]), "+v"(acc[1][3]), \
                                    "+v"(acc[2][0]), "+v"(acc[2][1]), "+v"(acc[2][2]), "+v"(acc[2][3]), "+v"(acc[3][0]), "+v"(acc[3][1]), "+v"(acc[3][2]), "+v"(acc[3][3]))
    bf16x8 cf0[4][2];
    BAR_C();
    rd(fa0, fb0, 0, 0);
    *(LAS u32x4*)(size_t)plo = pv[0];
    *(LAS u32x4*)(size_t)(plo + 1024) = pv[1];
    FENCE();
#pragma unroll
    for (int z = 0; z < 8; ++z) {
      rd(fa1, fb1, z, 1);
      mm(fa0, fb0, (z & 3) == 0);
      if (z == 4) asm volatile("" : "+v"(cf0[0][0]), "+v"(cf0[0][1]), "+v"(cf0[1][0]), "+v"(cf0[1][1]), "+v"(cf0[2][0]), "+v"(cf0[2][1]), "+v"(cf0[3][0]), "+v"(cf0[3][1]));
      SG_HALF16();
      PIN_ACC();
      if (z == 1) {
        asm volatile("s_waitcnt vmcnt(0)" ::: "memory");
        if (lane == 0) __hip_atomic_store(pflag_p + m * 4 + w, want, __ATOMIC_RELAXED, __HIP_MEMORY_SCOPE_AGENT);
      }
      BAR_C();
      if (z < 7) {
        rd(fa0, fb0, z + 1, 0);
        mm(fa1, fb1, false);
        if (z == 3) {
#pragma unroll
          for (int j = 0; j < 4; ++j) { cf0[j][0] = pk8(acc[j][0], acc[j][1]); cf0[j][1] = pk8(acc[j][2], acc[j][3]); }
          SG_MFMA(1); SG_DSR(2); SG_MFMA(1); SG_DSR(2);
#pragma unroll
          for (int i = 0; i < 8; ++i) { SG_MFMA(1); SG_DSR(1); }
#pragma unroll
          for (int i = 0; i < 6; ++i) { SG_MFMA(1); SG_VALU(2); }
          SG_VALU(20);
        } else {
          SG_HALF16();
          PIN_ACC();
        }
        FENCE();
      }
    }
    {
      bf16x8 pf[2][4][2][2], cf[4][2];
      auto rdP = [&](int G, int qt) {
#pragma unroll
        for (int i = 0; i < 4; ++i) pf[G][qt][i >> 1][i & 1] = ld128(pad[qt] + G * 16384 + (i >> 1) * 8192 + (i & 1) * 1024);
      };
      auto pc = [&](int G, int qt) {
#pragma unroll
        for (int i = 0; i < 4; ++i)
#pragma unroll
          for (int nt = 0; nt < 2; ++nt)
            o[nt][qt] = MFMA((G ? cf : cf0)[(i & 2) + nt][i & 1], pf[G][qt][i >> 1][i & 1], (G == 0 && i == 0) ? ZERO4 : o[nt][qt]);
      };
      auto pin = [&](int qt) { asm volatile("" : "+v"(o[0][qt]), "+v"(o[1][qt])); FENCE(); };
      auto send = [&](int qt) {
        __builtin_amdgcn_raw_buffer_store_b128(__builtin_bit_cast(u32x4, pk8(o[0][qt], o[1][qt])), rs, fo + ((gp ^ qt) << 14), 0, 16);
        FENCE();
      };
#define SG_PC(nr, nv) do { _Pragma("unroll") for (int i_ = 0; i_ < 4; ++i_) { SG_MFMA(1); if (nr) SG_DSR(1); SG_MFMA(1); if (nv) SG_VALU(nv); } } while (0)
#pragma unroll
      for (int j = 0; j < 4; ++j) {
#pragma unroll
        for (int i = 0; i < 4; ++i) acc[j][i] = MFMA(fa1[i], fb1[j], acc[j][i]);
        if (j == 0) rdP(0, 3);
        if (j == 1) rdP(0, 2);
        if (j == 2) rdP(1, 3);
        if (j == 3) rdP(1, 2);
#pragma unroll
        for (int i = 0; i < 4; ++i) { SG_MFMA(1); SG_DSR(1); }
        asm volatile("" : "+v"(acc[j][0]), "+v"(acc[j][1]), "+v"(acc[j][2]), "+v"(acc[j][3]));
        FENCE();
      }
      pc(0, 3); rdP(0, 1);
      cf[0][0] = pk8(acc[0][0], acc[0][1]); cf[0][1] = pk8(acc[0][2], acc[0][3]); cf[1][0] = pk8(acc[1][0], acc[1][1]); cf[1][1] = pk8(acc[1][2], acc[1][3]);
      SG_PC(1, 4);
      asm volatile("" : "+v"(cf[0][0]), "+v"(cf[0][1]), "+v"(cf[1][0]), "+v"(cf[1][1])); pin(3);
      pc(0, 2); rdP(1, 1);
      cf[2][0] = pk8(acc[2][0], acc[2][1]); cf[2][1] = pk8(acc[2][2], acc[2][3]); cf[3][0] = pk8(acc[3][0], acc[3][1]); cf[3][1] = pk8(acc[3][2], acc[3][3]);
      SG_PC(1, 4);
      asm volatile("" : "+v"(cf[2][0]), "+v"(cf[2][1]), "+v"(cf[3][0]), "+v"(cf[3][1])); pin(2);
      pc(1, 3); rdP(0, 0); SG_PC(1, 0); pin(3);
      pc(1, 2); rdP(1, 0); SG_PC(1, 0); pin(2);
      send(3);
      pc(0, 1); pin(1);
      send(2);
      pc(1, 1); pin(1);
      pc(0, 0); pin(0);
      send(1);
      pc(1, 0); pin(0);
    }
  }
  asm volatile("s_waitcnt vmcnt(0)" ::: "memory");
  if (lane < 4 && lane != gp) __hip_atomic_store(fs, want, __ATOMIC_RELAXED, __HIP_MEMORY_SCOPE_AGENT);
  {
    u32x4 d[3];
    for (unsigned spins = 0; spins < SPIN_CAP; ++spins) {
      const unsigned fl = __hip_atomic_load(fp, __ATOMIC_RELAXED, __HIP_MEMORY_SCOPE_AGENT);
      if (__all(fl == want || (lane & 3) == gp)) break;
    }
    asm volatile("" ::: "memory");
#pragma unroll
    for (int k = 0; k < 3; ++k) d[k] = __builtin_amdgcn_raw_buffer_load_b128(rs, fi, (gp ^ (k + 1)) * 4096, 16);
    float a[8];
#pragma unroll
    for (int i = 0; i < 4; ++i) { a[i] = o[0][0][i]; a[4 + i] = o[1][0][i]; }
#pragma unroll
    for (int k = 0; k < 3; ++k)
#pragma unroll
      for (int i = 0; i < 4; ++i) { a[2 * i] += __uint_as_float(d[k][i] << 16); a[2 * i + 1] += __uint_as_float(d[k][i] & 0xffff0000u); }
    const auto s0 = __builtin_amdgcn_permlane16_swap(pk(a[0], a[1]), pk(a[4], a[5]), false, false);
    const auto s1 = __builtin_amdgcn_permlane16_swap(pk(a[2], a[3]), pk(a[6], a[7]), false, false);
    *op = u32x4{s0[0], s1[0], s0[1], s1[1]};
    if (gp == 2 && m == 0 && w == 0 && lane == 0) __hip_atomic_store(epoch_p, want, __ATOMIC_RELAXED, __HIP_MEMORY_SCOPE_AGENT);
  }
}

extern "C" void kernel_launch(void* const* d_in, const int* in_sizes, int n_in, void* d_out, int out_size,
                              void* d_ws, size_t ws_size, hipStream_t stream) {
  Params p;
  p.x0 = (const char*)d_in[0]; p.k0 = (const char*)d_in[1]; p.v0 = (const char*)d_in[2]; p.wq0 = (const char*)d_in[3]; p.wo0 = (const char*)d_in[4];
  p.x1 = (const char*)d_in[5]; p.k1 = (const char*)d_in[6]; p.v1 = (const char*)d_in[7]; p.wq1 = (const char*)d_in[8]; p.wo1 = (const char*)d_in[9];
  p.out = (char*)d_out; p.ws = (char*)d_ws;
  gqa_fused<<<256, 512, 0, stream>>>(p);
}
```

```cpp
#include <hip/hip_runtime.h>
#include <hip/hip_bf16.h>

typedef short s16x4 __attribute__((ext_vector_type(4)));
typedef short bf16x8 __attribute__((ext_vector_type(8)));
typedef float f32x4 __attribute__((ext_vector_type(4)));
typedef float f32x2 __attribute__((ext_vector_type(2)));
typedef unsigned u32x4 __attribute__((ext_vector_type(4)));
typedef unsigned u32x2 __attribute__((ext_vector_type(2)));
typedef unsigned u32x16 __attribute__((ext_vector_type(16)));
typedef __bf16 bf16x2_t __attribute__((ext_vector_type(2)));
#define LAS __attribute__((address_space(3)))
#define GAS __attribute__((address_space(1)))
#define DI __device__ __forceinline__

struct Params {
  const char* x0; const char* wq0; const char* k0; const char* v0; const char* wo0; char* out0; char* ws0; char* ws0b;
  const char* x1; const char* wq1; const char* k1; const char* v1; const char* wo1; char* out1; char* ws1; char* ws1b;
};

constexpr unsigned WS_EPOCH = 0;
constexpr unsigned WS_PFLAG = 4096;
constexpr unsigned WS_FFLAG = 65536;
constexpr unsigned WS_P = 131072;
constexpr unsigned WS_F = WS_P + 32 * 65536;
constexpr unsigned WS_END = WS_F + 64 * 65536;

constexpr unsigned SW = 0;
constexpr unsigned SX = 131072;

DI unsigned pk(float a, float b) { f32x2 v = {a, b}; return __builtin_bit_cast(unsigned, __builtin_convertvector(v, bf16x2_t)); }
DI bf16x8 pk8(f32x4 a, f32x4 b) { u32x4 r = {pk(a[0], a[1]), pk(a[2], a[3]), pk(b[0], b[1]), pk(b[2], b[3])}; return __builtin_bit_cast(bf16x8, r); }
DI bf16x8 ld128(unsigned a) { return *(LAS const bf16x8*)(size_t)a; }
DI bf16x8 ldtr(unsigned a0, unsigned a1) {
  s16x4 lo = __builtin_amdgcn_ds_read_tr16_b64_v4i16((LAS s16x4*)(size_t)a0);
  s16x4 hi = __builtin_amdgcn_ds_read_tr16_b64_v4i16((LAS s16x4*)(size_t)a1);
  return __builtin_shufflevector(lo, hi, 0, 1, 2, 3, 4, 5, 6, 7);
}
#define MFMA(a, b, c) __builtin_amdgcn_mfma_f32_16x16x32_bf16((a), (b), (c), 0, 0, 0)
#define ZERO4 (f32x4{0.f, 0.f, 0.f, 0.f})
#define BAR_C() do { __builtin_amdgcn_sched_barrier(0); asm volatile("" ::: "memory"); __builtin_amdgcn_s_waitcnt(0xC07F); \
                     __builtin_amdgcn_s_barrier(); asm volatile("" ::: "memory"); __builtin_amdgcn_sched_barrier(0); } while (0)
#define BAR_L(n) asm volatile("s_waitcnt vmcnt(" #n ")\n\ts_barrier" ::: "memory")
#define SG_MFMA(n) __builtin_amdgcn_sched_group_barrier(0x008, (n), 0)
#define SG_DSR(n) __builtin_amdgcn_sched_group_barrier(0x100, (n), 0)
#define SG_DSW(n) __builtin_amdgcn_sched_group_barrier(0x200, (n), 0)
#define SG_VALU(n) __builtin_amdgcn_sched_group_barrier(0x002, (n), 0)
constexpr int THR = 11;
#define GL(n, pol) "s_waitcnt vmcnt(%[thr])\n\tglobal_load_lds_dwordx4 %" #n ", %[sb]" pol "\n\ts_add_u32 m0, m0, 0x1000\n\t"
DI void glds2(const char* sb, unsigned lds_addr, unsigned v0, unsigned v1) {
  asm volatile("s_mov_b32 m0, %[la]\n\t" GL(0, "") GL(1, "") :: "v"(v0), "v"(v1), [sb] "s"(sb), [la] "s"(lds_addr), [thr] "i"(THR) : "memory", "scc");
}
DI void glds4(const char* sb, unsigned lds_addr, const unsigned (&v)[4]) {
  asm volatile("s_mov_b32 m0, %[la]\n\t" GL(0, "") GL(1, "") GL(2, "") GL(3, "")
               :: "v"(v[0]), "v"(v[1]), "v"(v[2]), "v"(v[3]), [sb] "s"(sb), [la] "s"(lds_addr), [thr] "i"(THR) : "memory", "scc");
}
DI void glds8(const char* sb, unsigned lds_addr, const unsigned (&v)[8]) {
  asm volatile("s_mov_b32 m0, %[la]\n\t" GL(0, "") GL(1, "") GL(2, "") GL(3, "") GL(4, "") GL(5, "") GL(6, "") GL(7, "")
               :: "v"(v[0]), "v"(v[1]), "v"(v[2]), "v"(v[3]), "v"(v[4]), "v"(v[5]), "v"(v[6]), "v"(v[7]),
                  [sb] "s"(sb), [la] "s"(lds_addr), [thr] "i"(THR) : "memory", "scc");
}
DI void glds2_dev(const char* sb, unsigned lds_addr, unsigned v0, unsigned v1) {
  asm volatile("s_mov_b32 m0, %[la]\n\t" GL(0, " sc1") GL(1, " sc1") :: "v"(v0), "v"(v1), [sb] "s"(sb), [la] "s"(lds_addr), [thr] "i"(THR) : "memory", "scc");
}
DI unsigned next_epoch(unsigned e) { return e * 0x9E3779B1u + 0x7F4A7C15u; }
DI float max2(float a, float b) { return __builtin_elementwise_maximum(a, b); }
DI float max3(float a, float b, float c) { return max2(max2(a, b), c); }
DI float xmax(float v) {
  auto a = __builtin_amdgcn_permlane32_swap(__float_as_uint(v), __float_as_uint(v), false, false);
  v = max2(__uint_as_float(a[0]), __uint_as_float(a[1]));
  auto b = __builtin_amdgcn_permlane16_swap(__float_as_uint(v), __float_as_uint(v), false, false);
  return max2(__uint_as_float(b[0]), __uint_as_float(b[1]));
}
DI float xsum(float v) {
  auto a = __builtin_amdgcn_permlane32_swap(__float_as_uint(v), __float_as_uint(v), false, false);
  v = __uint_as_float(a[0]) + __uint_as_float(a[1]);
  auto b = __builtin_amdgcn_permlane16_swap(__float_as_uint(v), __float_as_uint(v), false, false);
  return __uint_as_float(b[0]) + __uint_as_float(b[1]);
}
constexpr int zslot(int z) { return (0x30213021 >> (4 * z)) & 3; }
constexpr int CODE_BYTES = 21504;
constexpr unsigned SPIN_CAP = 1u << 18;

__global__ __launch_bounds__(512, 2) void gqa_fused(Params P) {
  __shared__ __attribute__((aligned(1024))) char lds[163840];
  u32x16 ka;
  asm volatile("s_load_dwordx16 %0, %1, %2" : "=&s"(ka) : "s"(__builtin_amdgcn_kernarg_segment_ptr()), "s"((blockIdx.x & 4) * 16));
  __builtin_amdgcn_sched_barrier(0);
  const int tid = threadIdx.x, lane = tid & 63;
  const int w = __builtin_amdgcn_readfirstlane(tid >> 6);
  const int bid = blockIdx.x;
  const int xcd = bid & 7, sl = bid >> 3;
  const int br = xcd >> 2, gp = xcd & 3;
  const int bp = sl >> 3, m = sl & 7;
  const int hj = m & 3, h = gp * 4 + hj, b = bp * 2 + (m >> 2);
  const int team = xcd * 4 + bp, comp = br * 4 + bp, grp = comp * 8 + m;
  const unsigned lds0 = (unsigned)(size_t)(LAS char*)lds;
  const int k1 = (bp + m) & 3;
  {
    unsigned long long pc; asm volatile("s_getpc_b64 %0" : "=s"(pc));
    const unsigned vo = min((sl * 8 + w) * 128, CODE_BYTES - 128);
    asm volatile("s_mov_b32 m0, %2\n\ts_nop 0\n\tglobal_load_lds_dword %0, %1" :: "v"(vo), "s"(pc & ~127ull), "s"(lds0 + SX + 3 * 8192 + w * 256) : "memory");
  }
  const int lw = w & 3;
  const unsigned hi = lane >> 5, l31 = lane & 31;
  unsigned v_x = (lane >> 3) * 896 + (((lane & 7) ^ (4 * (lw & 1) + (lane >> 4))) * 16);
  unsigned v_x1 = v_x + 32 * 896;
  unsigned v_wq[8];
#pragma unroll
  for (int i = 0; i < 8; ++i)
    v_wq[i] = hi * 8192 + i * (8 * 8192) + (l31 ^ (((2 * (lw & 1) + hi) | ((i & 1) << 2)) << 1)) * 16;
  int sa = (b >> 1) * 2 + 2 * hj;
  sa = sa >= 7 ? sa - 7 : sa;
  unsigned ox = b * 57344 + lw * (8 * 896), owq = h * 512 + lw * (2 * 8192);
  unsigned ox0 = ox + sa * 128, owq0 = owq + sa * (64 * 8192), lx0 = lds0 + SX + lw * 1024;
  asm volatile("" : "+v"(v_x), "+v"(v_x1), "+v"(v_wq[0]), "+v"(v_wq[1]), "+v"(v_wq[2]), "+v"(v_wq[3]), "+v"(v_wq[4]), "+v"(v_wq[5]), "+v"(v_wq[6]), "+v"(v_wq[7]));
  asm volatile("s_waitcnt lgkmcnt(0)" : "+s"(ka), "+s"(sa), "+s"(ox0), "+s"(owq0), "+s"(lx0));
#define KPTR(lo, hi) ((char*)(((unsigned long long)(hi) << 32) | (lo)))
#define K_X KPTR(ka[0], ka[1])
#define K_WQ KPTR(ka[2], ka[3])
#define K_K KPTR(ka[4], ka[5])
#define K_V KPTR(ka[6], ka[7])
#define K_WO KPTR(ka[8], ka[9])
#define K_OUT KPTR(ka[10], ka[11])
#define K_WS KPTR(ka[12], ka[13])

  if (__builtin_expect(w >= 4, 1)) {
    glds2(K_X + ox0, lx0, v_x, v_x1);
    glds8(K_WQ + owq0, lds0 + SW + lw * 1024, v_wq);
    sa = sa == 6 ? 0 : sa + 1;
    __builtin_amdgcn_s_setprio(3);
    const char* const xb = K_X + ox;
    const char* const wqb = K_WQ + owq;
    auto stageA = [&](int slot) {
      glds2(xb + sa * 128, lds0 + SX + slot * 8192 + lw * 1024, v_x, v_x1);
      glds8(wqb + sa * (64 * 8192), lds0 + SW + slot * 32768 + lw * 1024, v_wq);
      sa = sa == 6 ? 0 : sa + 1;
    };
    char* const ws = K_WS;
    GAS unsigned* const epoch_p = (GAS unsigned*)(ws + WS_EPOCH + comp * 256);
    GAS unsigned* const pflag_p = (GAS unsigned*)(ws + WS_PFLAG + team * 256);
    unsigned epv;
    unsigned v_k[4], v_wo[4], v_v[4];
#pragma unroll
    for (int i = 0; i < 4; ++i) {
      v_k[i] = ((hi * 512 + ((l31 ^ (2 * lw + hi)) * 16)) ^ ((i & 1) * 128)) + i * 4096;
      v_wo[i] = (lane >> 3) * 896 + (((lane & 7) ^ ((((lane >> 4) & 1) | ((lw & 1) << 1)) << 1)) * 16) + (i >> 1) * 229376 + (i & 1) * (32 * 896);
      v_v[i] = (lane >> 3) * 512 + (((lane & 7) ^ (4 * (lw & 1) + (lane >> 4))) * 16) + (i >> 1) * (8 * 32768) + (i & 1) * 16384;
    }
    const char* const kb = K_K + (b * 8 + (h >> 1)) * 32768 + lw * 1024;
    const char* const vb = K_V + (bp * 16 + gp * 2) * 32768 + lw * 4096;
    const char* const wob = K_WO + gp * (4 * 229376) + m * 128 + lw * (8 * 896);
    auto stageK = [&]() {
      const int f = (h & 1) * 16384;
      glds4(kb + f, lds0 + SW + 3 * 32768 + lw * 1024 + f, v_k);
      glds4(kb + (f ^ 16384), lds0 + SW + 3 * 32768 + lw * 1024 + (f ^ 16384), v_k);
    };
    auto stageZ = [&](int z, int part) {
      const int gl = z >> 2, kq = (k1 + z) & 3;
      const unsigned d = lds0 + SW + zslot(z) * 32768 + lw * 1024;
      if (part & 1) glds4(wob + gl * (2 * 229376) + kq * (64 * 896), d, v_wo);
      if (part & 2) glds4(vb + gl * 32768 + kq * 128, d + 16384, v_v);
    };
    stageA(1);
    BAR_L(10);
    asm volatile("global_load_dword %0, %1, %2 sc1" : "=v"(epv) : "v"(0), "s"(epoch_p) : "memory");
    stageA(2);
#pragma unroll 1
    for (int t = 0; t < 4; ++t) {
      BAR_L(10);
      stageA(t == 3 ? 0 : t);
      if (m == 7 && t == 0) {
        const char* vt = vb + lw * 4096;
        asm volatile("s_mov_b32 m0, %3\n\ts_nop 0\n\tglobal_load_lds_dword %0, %1\n\tglobal_load_lds_dword %0, %2"
                     :: "v"(lane * 128), "s"(vt), "s"(vt + 8 * 32768), "s"(lds0 + SX + 3 * 8192 + 2048 + lw * 256) : "memory");
      }
    }
    asm volatile("" : "+v"(epv));
    const unsigned ep = __builtin_amdgcn_readfirstlane(epv);
    BAR_L(10); stageK();
    if (m == 7) {
      BAR_L(8); BAR_L(63); BAR_L(0);
      return;
    }
    BAR_L(8); stageZ(0, 1);
    BAR_L(63); stageZ(0, 2);
    BAR_L(8); stageZ(1, 3); stageZ(2, 1);
    BAR_L(12); stageZ(2, 2); stageZ(3, 1);
    BAR_L(12); stageZ(3, 2); stageZ(4, 1);
    BAR_L(12); stageZ(4, 2); stageZ(5, 1);
    unsigned fl;
    const unsigned voff_f = (lane & 31) * 4;
    const unsigned want = next_epoch(ep);
    auto stageP = [&](int k0, int k1_) {
      const char* src = ws + WS_P + team * 65536 + lw * 1024;
      const unsigned v_lin = lane * 16, v_lin1 = v_lin + 4096;
#pragma unroll
      for (int k = k0; k < k1_; ++k) {
        int mm = m + k; mm = k == 7 ? 7 : mm >= 7 ? mm - 7 : mm;
        glds2_dev(src + mm * 8192, lds0 + (((mm ^ m) & 4) ? SW + 1 * 32768 : SX) + (mm & 3) * 8192 + lw * 1024, v_lin, v_lin1);
      }
    };
#define LOAD_FLAGS() asm volatile("global_load_dword %0, %1, %2 sc1" : "=v"(fl) : "v"(voff_f), "s"(pflag_p) : "memory")
#define SPIN_FLAGS(ok) for (unsigned spins = 0; !__all(ok) && spins < SPIN_CAP; ++spins) \
      asm volatile("s_sleep 1\n\tglobal_load_dword %0, %1, %2 sc1\n\ts_waitcnt vmcnt(0)" : "=v"(fl) : "v"(voff_f), "s"(pflag_p) : "memory")
    BAR_L(12); stageZ(5, 2); stageZ(6, 1);
    BAR_L(12); LOAD_FLAGS();
    stageZ(6, 2); stageZ(7, 1);
    BAR_L(13); stageZ(7, 2);
    asm volatile("s_waitcnt vmcnt(12)" : "+v"(fl) :: "memory");
    SPIN_FLAGS(fl == want || (lane & 31) >= 28);
    stageP(1, 3);
    BAR_L(12); LOAD_FLAGS();
    stageP(3, 7);
    BAR_L(12);
    asm volatile("s_waitcnt vmcnt(8)" : "+v"(fl) :: "memory");
    SPIN_FLAGS(fl == want);
    stageP(7, 8);
    BAR_L(0);
    return;
  }

  const unsigned r16 = lane & 15, g = lane >> 4, q4 = r16 >> 2, p4 = lane & 3;
  char* const ws = K_WS;
  GAS unsigned* const epoch_p = (GAS unsigned*)(ws + WS_EPOCH + comp * 256);
  GAS unsigned* const pflag_p = (GAS unsigned*)(ws + WS_PFLAG + team * 256);
  asm volatile("s_waitcnt vmcnt(0)" ::: "memory");
  const __amdgpu_buffer_rsrc_t rs = __builtin_amdgcn_make_buffer_rsrc(ws, 0, WS_END, 0x00020000);
  const int ct = w & 1, bi = w >> 1;
  unsigned qa = lds0 + SX + w * 2048 + (g >> 1) * 256 + r16 * 16 + (g & 1) * 8;
  unsigned own = lds0 + SX + w * 8192 + lane * 16;
  unsigned kad[4];
#pragma unroll
  for (int i = 0; i < 4; ++i) kad[i] = (lds0 + SW + 3 * 32768 + r16 * 512 + ((g ^ r16) * 16)) ^ (i * 64);
  const unsigned po = w * 2048 + lane * 16;
  unsigned pgo = WS_P + team * 65536 + m * 8192 + po;
  unsigned plo = lds0 + SX + (m & 3) * 8192 + po;
  unsigned wba[2][2], vba[2][2], pad[4];
#pragma unroll
  for (int u = 0; u < 2; ++u)
#pragma unroll
    for (int i = 0; i < 2; ++i) {
      wba[u][i] = lds0 + SW + u * 65536 + (8 * g + q4) * 128 + (((4 * ct + 2 * i + (p4 >> 1)) ^ (((q4 >> 1) | ((g & 1) << 1)) << 1)) * 16) + (p4 & 1) * 8;
      vba[u][i] = lds0 + SW + u * 65536 + 16384 + bi * 8192 + r16 * 128 + (((4 * i + g) ^ ((r16 >> 1) & 7)) * 16);
    }
#pragma unroll
  for (int i = 0; i < 4; ++i) pad[i] = lds0 + (bi != (m >> 2) ? SW + 1 * 32768 : SX) + (gp ^ i) * 2048 + lane * 16;
  asm volatile("" : "+v"(wba[0][0]), "+v"(wba[0][1]), "+v"(wba[1][0]), "+v"(wba[1][1]), "+v"(vba[0][0]), "+v"(vba[0][1]), "+v"(vba[1][0]), "+v"(vba[1][1]),
                    "+v"(pad[0]), "+v"(pad[1]), "+v"(pad[2]), "+v"(pad[3]));
  unsigned fo = WS_F + grp * 65536 + gp * 4096 + w * 1024 + lane * 16;
  unsigned fi = WS_F + grp * 65536 + gp * 16384 + w * 1024 + lane * 16;
  GAS unsigned* const fflag_p = (GAS unsigned*)(ws + WS_FFLAG + grp * 1024 + w * 64);
  GAS unsigned* fp = fflag_p + gp * 64 + (lane & 3);
  GAS unsigned* fs = fflag_p + (lane & 3) * 64 + gp;
  GAS u32x4* op = (GAS u32x4*)(K_OUT + (((bp * 2 + bi) * 64 + gp * 16 + r16) * 896 + br * 448 + m * 64 + ct * 32 + (g & 1) * 16 + (g >> 1) * 8) * 2);
  asm volatile("" : "+v"(fo), "+v"(fi), "+v"(op), "+v"(fp), "+v"(fs));
  asm volatile("" : "+v"(qa), "+v"(own), "+v"(kad[0]), "+v"(kad[1]), "+v"(kad[2]), "+v"(kad[3]), "+v"(pgo), "+v"(plo));
  unsigned epv;
  {
    f32x4 qc[4][4];
    bf16x8 ga0[4], gb0[4], ga1[4], gb1[4];
    const unsigned xa = lds0 + SX + r16 * 128 + ((g ^ ((r16 >> 1) & 7)) * 16);
    const unsigned wa = lds0 + SW + (8 * g + q4) * 512 + (((8 * w + (p4 >> 1)) ^ ((q4 | ((g & 1) << 2)) << 1)) * 16) + (p4 & 1) * 8;
    unsigned xad[2] = {xa, xa ^ 64}, wad[2][4];
#pragma unroll
    for (int i = 0; i < 4; ++i) { wad[0][i] = wa ^ (i * 32); wad[1][i] = wad[0][i] + 65536; }
    asm volatile("" : "+v"(xad[0]), "+v"(xad[1]), "+v"(wad[0][0]), "+v"(wad[0][1]), "+v"(wad[0][2]), "+v"(wad[0][3]),
                      "+v"(wad[1][0]), "+v"(wad[1][1]), "+v"(wad[1][2]), "+v"(wad[1][3]));
    auto rd = [&](bf16x8 (&fa)[4], bf16x8 (&fb)[4], int slot, int half) {
#pragma unroll
      for (int i = 0; i < 4; ++i) {
        const unsigned wo_ = (slot & 1) * 32768 + half * 16384;
        fb[i] = ld128(xad[half] + slot * 8192 + i * 2048);
        fa[i] = ldtr(wad[slot >> 1][i] + wo_, wad[slot >> 1][i] + wo_ + 2048);
      }
    };
    auto mm16 = [&](const bf16x8 (&fa)[4], const bf16x8 (&fb)[4], bool first) {
#pragma unroll
      for (int i = 0; i < 4; ++i)
#pragma unroll
        for (int j = 0; j < 4; ++j) qc[i][j] = MFMA(fa[i], fb[j], first ? ZERO4 : qc[i][j]);
    };
#define SG_HALF16() do { SG_MFMA(1); SG_DSR(2); SG_MFMA(1); SG_DSR(2); _Pragma("unroll") for (int i_ = 0; i_ < 8; ++i_) { SG_MFMA(1); SG_DSR(1); } SG_MFMA(6); } while (0)
#define PIN_QC() asm volatile("" : "+v"(qc[0][0]), "+v"(qc[0][1]), "+v"(qc[0][2]), "+v"(qc[0][3]), "+v"(qc[1][0]), "+v"(qc[1][1]), "+v"(qc[1][2]), "+v"(qc[1][3]), \
                                   "+v"(qc[2][0]), "+v"(qc[2][1]), "+v"(qc[2][2]), "+v"(qc[2][3]), "+v"(qc[3][0]), "+v"(qc[3][1]), "+v"(qc[3][2]), "+v"(qc[3][3]))
    BAR_C();
    epv = __hip_atomic_load(epoch_p, __ATOMIC_RELAXED, __HIP_MEMORY_SCOPE_AGENT);
    rd(ga0, gb0, 0, 0);
    __builtin_amdgcn_sched_barrier(0);
#pragma unroll
    for (int t = 0; t < 7; ++t) {
      rd(ga1, gb1, t % 3, 1);
      mm16(ga0, gb0, t == 0);
      SG_HALF16();
      PIN_QC();
      BAR_C();
      if (t < 6) {
        rd(ga0, gb0, (t + 1) % 3, 0);
        mm16(ga1, gb1, false);
        SG_HALF16(); PIN_QC(); __builtin_amdgcn_sched_barrier(0);
      }
    }
#pragma unroll
    for (int i = 0; i <= 4; ++i) {
      if (i < 4) {
#pragma unroll
        for (int j = 0; j < 4; ++j) qc[i][j] = MFMA(ga1[i], gb1[j], qc[i][j]);
        asm volatile("" : "+v"(qc[i][0]), "+v"(qc[i][1]), "+v"(qc[i][2]), "+v"(qc[i][3]));
      }
      if (i > 0) {
#pragma unroll
        for (int j = 0; j < 4; ++j)
          *(LAS u32x2*)(size_t)(qa + j * 8192 + (i - 1) * 512) = u32x2{pk(qc[i - 1][j][0], qc[i - 1][j][1]), pk(qc[i - 1][j][2], qc[i - 1][j][3])};
        if (i < 4) { SG_MFMA(1); SG_VALU(2); SG_MFMA(1); SG_VALU(2); SG_DSW(1); SG_MFMA(1); SG_VALU(2); SG_DSW(1); SG_MFMA(1); SG_VALU(2); SG_DSW(2); }
      }
      __builtin_amdgcn_sched_barrier(0);
    }
  }
  BAR_C();
  const unsigned want = next_epoch(__builtin_amdgcn_readfirstlane(epv));
  u32x4 pv[2];
  {
    f32x4 sc[4];
    bf16x8 qf[8], kf[8][4];
    auto rdS = [&](int s) {
      qf[s] = ld128(own + s * 1024);
#pragma unroll
      for (int kt = 0; kt < 4; ++kt) kf[s][kt] = ld128(kad[s & 3] + kt * 8192 + (s >> 2) * 256);
      __builtin_amdgcn_sched_barrier(0);
    };
    rdS(0); rdS(1); rdS(2);
#pragma unroll
    for (int s = 0; s < 8; ++s) {
#pragma unroll
      for (int kt = 0; kt < 4; ++kt) sc[kt] = MFMA(kf[s][kt], qf[s], s == 0 ? ZERO4 : sc[kt]);
      asm volatile("" : "+v"(sc[0]), "+v"(sc[1]), "+v"(sc[2]), "+v"(sc[3]));
      __builtin_amdgcn_sched_barrier(0);
      if (s < 5) rdS(s + 3);
    }
    float mx = max3(max3(sc[0][0], sc[0][1], sc[0][2]), max3(sc[0][3], sc[1][0], sc[1][1]), max3(sc[1][2], sc[1][3], sc[2][0]));
    mx = max3(mx, max3(sc[2][1], sc[2][2], sc[2][3]), max3(max2(sc[3][0], sc[3][1]), sc[3][2], sc[3][3]));
    mx = xmax(mx);
    constexpr float C = 0.0625f * 1.4426950408889634f;
    const float mc = mx * C;
    float l = 0.f;
#pragma unroll
    for (int kt = 0; kt < 4; ++kt)
#pragma unroll
      for (int r = 0; r < 4; ++r) { sc[kt][r] = __builtin_amdgcn_exp2f(fmaf(sc[kt][r], C, -mc)); l += sc[kt][r]; }
    l = xsum(l);
    const float inv = __builtin_amdgcn_rcpf(l);
#pragma unroll
    for (int e = 0; e < 2; ++e) {
      pv[e] = __builtin_bit_cast(u32x4, pk8(sc[2 * e] * inv, sc[2 * e + 1] * inv));
      __builtin_amdgcn_raw_buffer_store_b128(pv[e], rs, pgo + e * 1024, 0, 16);
    }
  }
  if (m == 7) {
    asm volatile("s_waitcnt vmcnt(0)" ::: "memory");
    if (lane == 0) __hip_atomic_store(pflag_p + m * 4 + w, want, __ATOMIC_RELAXED, __HIP_MEMORY_SCOPE_AGENT);
    return;
  }
#define FENCE() __builtin_amdgcn_sched_barrier(0)
  f32x4 o[2][4];
  {
    f32x4 acc[4][4];
    bf16x8 fa0[4], fb0[4], fa1[4], fb1[4];
    auto rd = [&](bf16x8 (&fa)[4], bf16x8 (&fb)[4], int z, int half) {
      const int slot = zslot(z);
#pragma unroll
      for (int i = 0; i < 4; ++i) {
        const unsigned lo_ = (slot & 1) * 32768;
        fa[i] = ld128(vba[slot >> 1][half] + lo_ + i * 2048);
        const unsigned wa_ = wba[slot >> 1][i & 1] + lo_ + (i >> 1) * 8192 + half * 4096;
        fb[i] = ldtr(wa_, wa_ + 512);
      }
    };
    auto mm = [&](const bf16x8 (&fa)[4], const bf16x8 (&fb)[4], bool first) {
#pragma unroll
      for (int j = 0; j < 4; ++j)
#pragma unroll
        for (int i = 0; i < 4; ++i) acc[j][i] = MFMA(fa[i], fb[j], first ? ZERO4 : acc[j][i]);
    };
#define PIN_ACC() asm volatile("" : "+v"(acc[0][0]), "+v"(acc[0][1]), "+v"(acc[0][2]), "+v"(acc[0][3]), "+v"(acc[1][0]), "+v"(acc[1][1]), "+v"(acc[1][2]), "+v"(acc[1][3]), \
                                    "+v"(acc[2][0]), "+v"(acc[2][1]), "+v"(acc[2][2]), "+v"(acc[2][3]), "+v"(acc[3][0]), "+v"(acc[3][1]), "+v"(acc[3][2]), "+v"(acc[3][3]))
    bf16x8 cf0[4][2];
    BAR_C();
    rd(fa0, fb0, 0, 0);
    *(LAS u32x4*)(size_t)plo = pv[0];
    *(LAS u32x4*)(size_t)(plo + 1024) = pv[1];
    FENCE();
#pragma unroll
    for (int z = 0; z < 8; ++z) {
      rd(fa1, fb1, z, 1);
      mm(fa0, fb0, (z & 3) == 0);
      if (z == 4) asm volatile("" : "+v"(cf0[0][0]), "+v"(cf0[0][1]), "+v"(cf0[1][0]), "+v"(cf0[1][1]), "+v"(cf0[2][0]), "+v"(cf0[2][1]), "+v"(cf0[3][0]), "+v"(cf0[3][1]));
      SG_HALF16();
      PIN_ACC();
      if (z == 1) {
        asm volatile("s_waitcnt vmcnt(0)" ::: "memory");
        if (lane == 0) __hip_atomic_store(pflag_p + m * 4 + w, want, __ATOMIC_RELAXED, __HIP_MEMORY_SCOPE_AGENT);
      }
      BAR_C();
      if (z < 7) {
        rd(fa0, fb0, z + 1, 0);
        mm(fa1, fb1, false);
        if (z == 3) {
#pragma unroll
          for (int j = 0; j < 4; ++j) { cf0[j][0] = pk8(acc[j][0], acc[j][1]); cf0[j][1] = pk8(acc[j][2], acc[j][3]); }
          SG_MFMA(1); SG_DSR(2); SG_MFMA(1); SG_DSR(2);
#pragma unroll
          for (int i = 0; i < 8; ++i) { SG_MFMA(1); SG_DSR(1); }
#pragma unroll
          for (int i = 0; i < 6; ++i) { SG_MFMA(1); SG_VALU(2); }
          SG_VALU(20);
        } else {
          SG_HALF16();
          PIN_ACC();
        }
        FENCE();
      }
    }
    {
      bf16x8 pf[2][4][2][2], cf[4][2];
      auto rdP = [&](int G, int qt) {
#pragma unroll
        for (int i = 0; i < 4; ++i) pf[G][qt][i >> 1][i & 1] = ld128(pad[qt] + G * 16384 + (i >> 1) * 8192 + (i & 1) * 1024);
      };
      auto pc = [&](int G, int qt) {
#pragma unroll
        for (int i = 0; i < 4; ++i)
#pragma unroll
          for (int nt = 0; nt < 2; ++nt)
            o[nt][qt] = MFMA((G ? cf : cf0)[(i & 2) + nt][i & 1], pf[G][qt][i >> 1][i & 1], (G == 0 && i == 0) ? ZERO4 : o[nt][qt]);
      };
      auto pin = [&](int qt) { asm volatile("" : "+v"(o[0][qt]), "+v"(o[1][qt])); FENCE(); };
      auto send = [&](int qt) {
        __builtin_amdgcn_raw_buffer_store_b128(__builtin_bit_cast(u32x4, pk8(o[0][qt], o[1][qt])), rs, fo + ((gp ^ qt) << 14), 0, 16);
        FENCE();
      };
#define SG_PC(nr, nv) do { _Pragma("unroll") for (int i_ = 0; i_ < 4; ++i_) { SG_MFMA(1); if (nr) SG_DSR(1); SG_MFMA(1); if (nv) SG_VALU(nv); } } while (0)
#pragma unroll
      for (int j = 0; j < 4; ++j) {
#pragma unroll
        for (int i = 0; i < 4; ++i) acc[j][i] = MFMA(fa1[i], fb1[j], acc[j][i]);
        if (j == 0) rdP(0, 3);
        if (j == 1) rdP(0, 2);
        if (j == 2) rdP(1, 3);
        if (j == 3) rdP(1, 2);
#pragma unroll
        for (int i = 0; i < 4; ++i) { SG_MFMA(1); SG_DSR(1); }
        asm volatile("" : "+v"(acc[j][0]), "+v"(acc[j][1]), "+v"(acc[j][2]), "+v"(acc[j][3]));
        FENCE();
      }
      pc(0, 3); rdP(0, 1);
      cf[0][0] = pk8(acc[0][0], acc[0][1]); cf[0][1] = pk8(acc[0][2], acc[0][3]); cf[1][0] = pk8(acc[1][0], acc[1][1]); cf[1][1] = pk8(acc[1][2], acc[1][3]);
      SG_PC(1, 4);
      asm volatile("" : "+v"(cf[0][0]), "+v"(cf[0][1]), "+v"(cf[1][0]), "+v"(cf[1][1])); pin(3);
      pc(0, 2); rdP(1, 1);
      cf[2][0] = pk8(acc[2][0], acc[2][1]); cf[2][1] = pk8(acc[2][2], acc[2][3]); cf[3][0] = pk8(acc[3][0], acc[3][1]); cf[3][1] = pk8(acc[3][2], acc[3][3]);
      SG_PC(1, 4);
      asm volatile("" : "+v"(cf[2][0]), "+v"(cf[2][1]), "+v"(cf[3][0]), "+v"(cf[3][1])); pin(2);
      pc(1, 3); rdP(0, 0); SG_PC(1, 0); pin(3);
      pc(1, 2); rdP(1, 0); SG_PC(1, 0); pin(2);
      send(3);
      pc(0, 1); pin(1);
      send(2);
      pc(1, 1); pin(1);
      pc(0, 0); pin(0);
      send(1);
      pc(1, 0); pin(0);
    }
  }
  asm volatile("s_waitcnt vmcnt(0)" ::: "memory");
  if (lane < 4 && lane != gp) __hip_atomic_store(fs, want, __ATOMIC_RELAXED, __HIP_MEMORY_SCOPE_AGENT);
  {
    u32x4 d[3];
    for (unsigned spins = 0; spins < SPIN_CAP; ++spins) {
      const unsigned fl = __hip_atomic_load(fp, __ATOMIC_RELAXED, __HIP_MEMORY_SCOPE_AGENT);
      if (__all(fl == want || (lane & 3) == gp)) break;
    }
    asm volatile("" ::: "memory");
#pragma unroll
    for (int k = 0; k < 3; ++k) d[k] = __builtin_amdgcn_raw_buffer_load_b128(rs, fi, (gp ^ (k + 1)) * 4096, 16);
    float a[8];
#pragma unroll
    for (int i = 0; i < 4; ++i) { a[i] = o[0][0][i]; a[4 + i] = o[1][0][i]; }
#pragma unroll
    for (int k = 0; k < 3; ++k)
#pragma unroll
      for (int i = 0; i < 4; ++i) { a[2 * i] += __uint_as_float(d[k][i] << 16); a[2 * i + 1] += __uint_as_float(d[k][i] & 0xffff0000u); }
    const auto s0 = __builtin_amdgcn_permlane16_swap(pk(a[0], a[1]), pk(a[4], a[5]), false, false);
    const auto s1 = __builtin_amdgcn_permlane16_swap(pk(a[2], a[3]), pk(a[6], a[7]), false, false);
    *op = u32x4{s0[0], s1[0], s0[1], s1[1]};
    if (gp == 2 && m == 0 && w == 0 && lane == 0) __hip_atomic_store(epoch_p, want, __ATOMIC_RELAXED, __HIP_MEMORY_SCOPE_AGENT);
  }
}

extern "C" void kernel_launch(void* const* d_in, const int* in_sizes, int n_in, void* d_out, int out_size,
                              void* d_ws, size_t ws_size, hipStream_t stream) {
  Params p;
  p.x0 = (const char*)d_in[0]; p.k0 = (const char*)d_in[1]; p.v0 = (const char*)d_in[2]; p.wq0 = (const char*)d_in[3]; p.wo0 = (const char*)d_in[4];
  p.x1 = (const char*)d_in[5]; p.k1 = (const char*)d_in[6]; p.v1 = (const char*)d_in[7]; p.wq1 = (const char*)d_in[8]; p.wo1 = (const char*)d_in[9];
  p.out0 = p.out1 = (char*)d_out;
  p.ws0 = p.ws0b = p.ws1 = p.ws1b = (char*)d_ws;
  gqa_fused<<<256, 512, 0, stream>>>(p);
}
```

```cpp
#include <hip/hip_runtime.h>
#include <hip/hip_bf16.h>

typedef short s16x4 __attribute__((ext_vector_type(4)));
typedef short bf16x8 __attribute__((ext_vector_type(8)));
typedef float f32x4 __attribute__((ext_vector_type(4)));
typedef float f32x2 __attribute__((ext_vector_type(2)));
typedef unsigned u32x4 __attribute__((ext_vector_type(4)));
typedef unsigned u32x2 __attribute__((ext_vector_type(2)));
typedef unsigned u32x16 __attribute__((ext_vector_type(16)));
typedef __bf16 bf16x2_t __attribute__((ext_vector_type(2)));
#define LAS __attribute__((address_space(3)))
#define GAS __attribute__((address_space(1)))
#define DI __device__ __forceinline__

struct Params {
  const char* x0; const char* wq0; const char* k0; const char* v0; const char* wo0; char* out0; char* ws0; char* ws0b;
  const char* x1; const char* wq1; const char* k1; const char* v1; const char* wo1; char* out1; char* ws1; char* ws1b;
};

constexpr unsigned WS_EPOCH = 0;
constexpr unsigned WS_PFLAG = 4096;
constexpr unsigned WS_FFLAG = 65536;
constexpr unsigned WS_P = 131072;
constexpr unsigned WS_F = WS_P + 32 * 65536;
constexpr unsigned WS_END = WS_F + 64 * 65536;

constexpr unsigned SW = 0;
constexpr unsigned SX = 131072;

DI unsigned pk(float a, float b) { f32x2 v = {a, b}; return __builtin_bit_cast(unsigned, __builtin_convertvector(v, bf16x2_t)); }
DI bf16x8 pk8(f32x4 a, f32x4 b) { u32x4 r = {pk(a[0], a[1]), pk(a[2], a[3]), pk(b[0], b[1]), pk(b[2], b[3])}; return __builtin_bit_cast(bf16x8, r); }
DI bf16x8 ld128(unsigned a) { return *(LAS const bf16x8*)(size_t)a; }
DI bf16x8 ldtr(unsigned a0, unsigned a1) {
  s16x4 lo = __builtin_amdgcn_ds_read_tr16_b64_v4i16((LAS s16x4*)(size_t)a0);
  s16x4 hi = __builtin_amdgcn_ds_read_tr16_b64_v4i16((LAS s16x4*)(size_t)a1);
  return __builtin_shufflevector(lo, hi, 0, 1, 2, 3, 4, 5, 6, 7);
}
#define MFMA(a, b, c) __builtin_amdgcn_mfma_f32_16x16x32_bf16((a), (b), (c), 0, 0, 0)
#define ZERO4 (f32x4{0.f, 0.f, 0.f, 0.f})
#define BAR_C() do { __builtin_amdgcn_sched_barrier(0); asm volatile("" ::: "memory"); __builtin_amdgcn_s_waitcnt(0xC07F); \
                     __builtin_amdgcn_s_barrier(); asm volatile("" ::: "memory"); __builtin_amdgcn_sched_barrier(0); } while (0)
#define BAR_L(n) asm volatile("s_waitcnt vmcnt(" #n ")\n\ts_barrier" ::: "memory")
#define SG_MFMA(n) __builtin_amdgcn_sched_group_barrier(0x008, (n), 0)
#define SG_DSR(n) __builtin_amdgcn_sched_group_barrier(0x100, (n), 0)
#define SG_DSW(n) __builtin_amdgcn_sched_group_barrier(0x200, (n), 0)
#define SG_VALU(n) __builtin_amdgcn_sched_group_barrier(0x002, (n), 0)
constexpr int THR = 11;
#define GL(n, pol) "s_waitcnt vmcnt(%[thr])\n\tglobal_load_lds_dwordx4 %" #n ", %[sb]" pol "\n\ts_add_u32 m0, m0, 0x1000\n\t"
DI void glds2(const char* sb, unsigned lds_addr, unsigned v0, unsigned v1) {
  asm volatile("s_mov_b32 m0, %[la]\n\t" GL(0, "") GL(1, "") :: "v"(v0), "v"(v1), [sb] "s"(sb), [la] "s"(lds_addr), [thr] "i"(THR) : "memory", "scc");
}
DI void glds4(const char* sb, unsigned lds_addr, const unsigned (&v)[4]) {
  asm volatile("s_mov_b32 m0, %[la]\n\t" GL(0, "") GL(1, "") GL(2, "") GL(3, "")
               :: "v"(v[0]), "v"(v[1]), "v"(v[2]), "v"(v[3]), [sb] "s"(sb), [la] "s"(lds_addr), [thr] "i"(THR) : "memory", "scc");
}
DI void glds8(const char* sb, unsigned lds_addr, const unsigned (&v)[8]) {
  asm volatile("s_mov_b32 m0, %[la]\n\t" GL(0, "") GL(1, "") GL(2, "") GL(3, "") GL(4, "") GL(5, "") GL(6, "") GL(7, "")
               :: "v"(v[0]), "v"(v[1]), "v"(v[2]), "v"(v[3]), "v"(v[4]), "v"(v[5]), "v"(v[6]), "v"(v[7]),
                  [sb] "s"(sb), [la] "s"(lds_addr), [thr] "i"(THR) : "memory", "scc");
}
DI void glds2_dev(const char* sb, unsigned lds_addr, unsigned v0, unsigned v1) {
  asm volatile("s_mov_b32 m0, %[la]\n\t" GL(0, " sc1") GL(1, " sc1") :: "v"(v0), "v"(v1), [sb] "s"(sb), [la] "s"(lds_addr), [thr] "i"(THR) : "memory", "scc");
}
DI unsigned next_epoch(unsigned e) { return e * 0x9E3779B1u + 0x7F4A7C15u; }
DI float max2(float a, float b) { return __builtin_elementwise_maximum(a, b); }
DI float max3(float a, float b, float c) { return max2(max2(a, b), c); }
DI float xmax(float v) {
  auto a = __builtin_amdgcn_permlane32_swap(__float_as_uint(v), __float_as_uint(v), false, false);
  v = max2(__uint_as_float(a[0]), __uint_as_float(a[1]));
  auto b = __builtin_amdgcn_permlane16_swap(__float_as_uint(v), __float_as_uint(v), false, false);
  return max2(__uint_as_float(b[0]), __uint_as_float(b[1]));
}
DI float xsum(float v) {
  auto a = __builtin_amdgcn_permlane32_swap(__float_as_uint(v), __float_as_uint(v), false, false);
  v = __uint_as_float(a[0]) + __uint_as_float(a[1]);
  auto b = __builtin_amdgcn_permlane16_swap(__float_as_uint(v), __float_as_uint(v), false, false);
  return __uint_as_float(b[0]) + __uint_as_float(b[1]);
}
constexpr int zslot(int z) { return (0x30213021 >> (4 * z)) & 3; }
constexpr int CODE_BYTES = 21888;
constexpr unsigned SPIN_CAP = 1u << 18;

__global__ __launch_bounds__(512, 2) void gqa_fused(Params P) {
  __shared__ __attribute__((aligned(1024))) char lds[163840];
  u32x16 ka;
  asm volatile("s_load_dwordx16 %0, %1, %2" : "=&s"(ka) : "s"(__builtin_amdgcn_kernarg_segment_ptr()), "s"((blockIdx.x & 4) * 16));
  __builtin_amdgcn_sched_barrier(0);
  const int tid = threadIdx.x, lane = tid & 63;
  const int w = __builtin_amdgcn_readfirstlane(tid >> 6);
  const int bid = blockIdx.x;
  const int xcd = bid & 7, sl = bid >> 3;
  const int br = xcd >> 2, gp = xcd & 3;
  const int bp = sl >> 3, m = sl & 7;
  const int hj = m & 3, h = gp * 4 + hj, b = bp * 2 + (m >> 2);
  const int team = xcd * 4 + bp, comp = br * 4 + bp, grp = comp * 8 + m;
  const unsigned lds0 = (unsigned)(size_t)(LAS char*)lds;
  const int k1 = (bp + m) & 3;
  {
    unsigned long long pc; asm volatile("s_getpc_b64 %0" : "=s"(pc));
    const unsigned vo = min((sl * 8 + w) * 128, CODE_BYTES - 128);
    asm volatile("s_mov_b32 m0, %2\n\ts_nop 0\n\tglobal_load_lds_dword %0, %1" :: "v"(vo), "s"(pc & ~127ull), "s"(lds0 + SX + 3 * 8192 + w * 256) : "memory");
  }
  const int lw = w & 3;
  const unsigned hi = lane >> 5, l31 = lane & 31;
  unsigned v_x = (lane >> 3) * 896 + (((lane & 7) ^ (4 * (lw & 1) + (lane >> 4))) * 16);
  unsigned v_x1 = v_x + 32 * 896;
  unsigned v_wq[8];
#pragma unroll
  for (int i = 0; i < 8; ++i)
    v_wq[i] = hi * 8192 + i * (8 * 8192) + (l31 ^ (((2 * (lw & 1) + hi) | ((i & 1) << 2)) << 1)) * 16;
  int sa = (b >> 1) * 2 + 2 * hj;
  sa = sa >= 7 ? sa - 7 : sa;
  unsigned ox = b * 57344 + lw * (8 * 896), owq = h * 512 + lw * (2 * 8192);
  unsigned ox0 = ox + sa * 128, owq0 = owq + sa * (64 * 8192), lx0 = lds0 + SX + lw * 1024;
  asm volatile("" : "+v"(v_x), "+v"(v_x1), "+v"(v_wq[0]), "+v"(v_wq[1]), "+v"(v_wq[2]), "+v"(v_wq[3]), "+v"(v_wq[4]), "+v"(v_wq[5]), "+v"(v_wq[6]), "+v"(v_wq[7]));
  asm volatile("s_waitcnt lgkmcnt(0)" : "+s"(ka), "+s"(sa), "+s"(ox0), "+s"(owq0), "+s"(lx0));
#define KPTR(lo, hi) ((char*)(((unsigned long long)(hi) << 32) | (lo)))
#define K_X KPTR(ka[0], ka[1])
#define K_WQ KPTR(ka[2], ka[3])
#define K_K KPTR(ka[4], ka[5])
#define K_V KPTR(ka[6], ka[7])
#define K_WO KPTR(ka[8], ka[9])
#define K_OUT KPTR(ka[10], ka[11])
#define K_WS KPTR(ka[12], ka[13])

  if (__builtin_expect(w >= 4, 1)) {
    glds2(K_X + ox0, lx0, v_x, v_x1);
    glds8(K_WQ + owq0, lds0 + SW + lw * 1024, v_wq);
    sa = sa == 6 ? 0 : sa + 1;
    __builtin_amdgcn_s_setprio(3);
    const char* const xb = K_X + ox;
    const char* const wqb = K_WQ + owq;
    auto stageA = [&](int slot) {
      glds2(xb + sa * 128, lds0 + SX + slot * 8192 + lw * 1024, v_x, v_x1);
      glds8(wqb + sa * (64 * 8192), lds0 + SW + slot * 32768 + lw * 1024, v_wq);
      sa = sa == 6 ? 0 : sa + 1;
    };
    char* const ws = K_WS;
    GAS unsigned* const epoch_p = (GAS unsigned*)(ws + WS_EPOCH + comp * 256);
    GAS unsigned* const pflag_p = (GAS unsigned*)(ws + WS_PFLAG + team * 256);
    unsigned epv;
    unsigned v_k[4], v_wo[4], v_v[4];
#pragma unroll
    for (int i = 0; i < 4; ++i) {
      v_k[i] = ((hi * 512 + ((l31 ^ (2 * lw + hi)) * 16)) ^ ((i & 1) * 128)) + i * 4096;
      v_wo[i] = (lane >> 3) * 896 + (((lane & 7) ^ ((((lane >> 4) & 1) | ((lw & 1) << 1)) << 1)) * 16) + (i >> 1) * 229376 + (i & 1) * (32 * 896);
      v_v[i] = (lane >> 3) * 512 + (((lane & 7) ^ (4 * (lw & 1) + (lane >> 4))) * 16) + (i >> 1) * (8 * 32768) + (i & 1) * 16384;
    }
    const char* const kb = K_K + (b * 8 + (h >> 1)) * 32768 + lw * 1024;
    const char* const vb = K_V + (bp * 16 + gp * 2) * 32768 + lw * 4096;
    const char* const wob = K_WO + gp * (4 * 229376) + m * 128 + lw * (8 * 896);
    auto stageK = [&]() {
      const int f = (h & 1) * 16384;
      glds4(kb + f, lds0 + SW + 3 * 32768 + lw * 1024 + f, v_k);
      glds4(kb + (f ^ 16384), lds0 + SW + 3 * 32768 + lw * 1024 + (f ^ 16384), v_k);
    };
    auto stageZ = [&](int z, int part) {
      const int gl = z >> 2, kq = (k1 + z) & 3;
      const unsigned d = lds0 + SW + zslot(z) * 32768 + lw * 1024;
      if (part & 1) glds4(wob + gl * (2 * 229376) + kq * (64 * 896), d, v_wo);
      if (part & 2) glds4(vb + gl * 32768 + kq * 128, d + 16384, v_v);
    };
    stageA(1);
    BAR_L(10);
    asm volatile("global_load_dword %0, %1, %2 sc1" : "=v"(epv) : "v"(0), "s"(epoch_p) : "memory");
    stageA(2);
#pragma unroll 1
    for (int t = 0; t < 4; ++t) {
      BAR_L(10);
      stageA(t == 3 ? 0 : t);
      if (m == 7 && t == 0) {
        const char* vt = vb + lw * 4096;
        asm volatile("s_mov_b32 m0, %3\n\ts_nop 0\n\tglobal_load_lds_dword %0, %1\n\tglobal_load_lds_dword %0, %2"
                     :: "v"(lane * 128), "s"(vt), "s"(vt + 8 * 32768), "s"(lds0 + SX + 3 * 8192 + 2048 + lw * 256) : "memory");
      }
    }
    asm volatile("" : "+v"(epv));
    const unsigned ep = __builtin_amdgcn_readfirstlane(epv);
    BAR_L(10); stageK();
    if (m == 7) {
      BAR_L(8); BAR_L(63); BAR_L(0);
      return;
    }
    BAR_L(8); stageZ(0, 1);
    BAR_L(63); stageZ(0, 2);
    BAR_L(8); stageZ(1, 3); stageZ(2, 1);
    BAR_L(12); stageZ(2, 2); stageZ(3, 1);
    BAR_L(12); stageZ(3, 2); stageZ(4, 1);
    BAR_L(12); stageZ(4, 2); stageZ(5, 1);
    unsigned fl;
    const unsigned voff_f = (lane & 31) * 4;
    const unsigned want = next_epoch(ep);
    auto stageP = [&](int k0, int k1_) {
      const char* src = ws + WS_P + team * 65536 + lw * 1024;
      const unsigned v_lin = lane * 16, v_lin1 = v_lin + 4096;
#pragma unroll
      for (int k = k0; k < k1_; ++k) {
        int mm = m + k; mm = k == 7 ? 7 : mm >= 7 ? mm - 7 : mm;
        glds2_dev(src + mm * 8192, lds0 + (((mm ^ m) & 4) ? SW + 1 * 32768 : SX) + (mm & 3) * 8192 + lw * 1024, v_lin, v_lin1);
      }
    };
#define LOAD_FLAGS() asm volatile("global_load_dword %0, %1, %2 sc1" : "=v"(fl) : "v"(voff_f), "s"(pflag_p) : "memory")
#define SPIN_FLAGS(ok) for (unsigned spins = 0; !__all(ok) && spins < SPIN_CAP; ++spins) \
      asm volatile("s_sleep 1\n\tglobal_load_dword %0, %1, %2 sc1\n\ts_waitcnt vmcnt(0)" : "=v"(fl) : "v"(voff_f), "s"(pflag_p) : "memory")
    BAR_L(12); stageZ(5, 2); stageZ(6, 1);
    BAR_L(12); LOAD_FLAGS();
    stageZ(6, 2); stageZ(7, 1);
    BAR_L(13); stageZ(7, 2);
    asm volatile("s_waitcnt vmcnt(12)" : "+v"(fl) :: "memory");
    SPIN_FLAGS(fl == want || (lane & 31) >= 28);
    stageP(1, 3);
    BAR_L(12); LOAD_FLAGS();
    stageP(3, 7);
    BAR_L(12);
    asm volatile("s_waitcnt vmcnt(8)" : "+v"(fl) :: "memory");
    SPIN_FLAGS(fl == want);
    stageP(7, 8);
    BAR_L(0);
    return;
  }

  const unsigned r16 = lane & 15, g = lane >> 4, q4 = r16 >> 2, p4 = lane & 3;
  char* const ws = K_WS;
  GAS unsigned* const epoch_p = (GAS unsigned*)(ws + WS_EPOCH + comp * 256);
  GAS unsigned* const pflag_p = (GAS unsigned*)(ws + WS_PFLAG + team * 256);
  asm volatile("s_waitcnt vmcnt(0)" ::: "memory");
  const __amdgpu_buffer_rsrc_t rs = __builtin_amdgcn_make_buffer_rsrc(ws, 0, WS_END, 0x00020000);
  const int ct = w & 1, bi = w >> 1;
  unsigned qa = lds0 + SX + w * 2048 + (g >> 1) * 256 + r16 * 16 + (g & 1) * 8;
  unsigned own = lds0 + SX + w * 8192 + lane * 16;
  unsigned kad[4];
#pragma unroll
  for (int i = 0; i < 4; ++i) kad[i] = (lds0 + SW + 3 * 32768 + r16 * 512 + ((g ^ r16) * 16)) ^ (i * 64);
  const unsigned po = w * 2048 + lane * 16;
  unsigned pgo = WS_P + team * 65536 + m * 8192 + po;
  unsigned plo = lds0 + SX + (m & 3) * 8192 + po;
  unsigned wba[2][2], vba[2][2], pad[4];
#pragma unroll
  for (int u = 0; u < 2; ++u)
#pragma unroll
    for (int i = 0; i < 2; ++i) {
      wba[u][i] = lds0 + SW + u * 65536 + (8 * g + q4) * 128 + (((4 * ct + 2 * i + (p4 >> 1)) ^ (((q4 >> 1) | ((g & 1) << 1)) << 1)) * 16) + (p4 & 1) * 8;
      vba[u][i] = lds0 + SW + u * 65536 + 16384 + bi * 8192 + r16 * 128 + (((4 * i + g) ^ ((r16 >> 1) & 7)) * 16);
    }
#pragma unroll
  for (int i = 0; i < 4; ++i) pad[i] = lds0 + (bi != (m >> 2) ? SW + 1 * 32768 : SX) + (gp ^ i) * 2048 + lane * 16;
  asm volatile("" : "+v"(wba[0][0]), "+v"(wba[0][1]), "+v"(wba[1][0]), "+v"(wba[1][1]), "+v"(vba[0][0]), "+v"(vba[0][1]), "+v"(vba[1][0]), "+v"(vba[1][1]),
                    "+v"(pad[0]), "+v"(pad[1]), "+v"(pad[2]), "+v"(pad[3]));
  unsigned fo = WS_F + grp * 65536 + gp * 4096 + w * 1024 + lane * 16;
  unsigned fi = WS_F + grp * 65536 + gp * 16384 + w * 1024 + lane * 16;
  GAS unsigned* const fflag_p = (GAS unsigned*)(ws + WS_FFLAG + grp * 1024 + w * 64);
  GAS unsigned* fp = fflag_p + gp * 64 + (lane & 3);
  GAS unsigned* fs = fflag_p + (lane & 3) * 64 + gp;
  GAS u32x4* op = (GAS u32x4*)(K_OUT + (((bp * 2 + bi) * 64 + gp * 16 + r16) * 896 + br * 448 + m * 64 + ct * 32 + (g & 1) * 16 + (g >> 1) * 8) * 2);
  asm volatile("" : "+v"(fo), "+v"(fi), "+v"(op), "+v"(fp), "+v"(fs));
  asm volatile("" : "+v"(qa), "+v"(own), "+v"(kad[0]), "+v"(kad[1]), "+v"(kad[2]), "+v"(kad[3]), "+v"(pgo), "+v"(plo));
  unsigned epv;
  {
    f32x4 qc[4][4];
    bf16x8 ga0[4], gb0[4], ga1[4], gb1[4];
    const unsigned xa = lds0 + SX + r16 * 128 + ((g ^ ((r16 >> 1) & 7)) * 16);
    const unsigned wa = lds0 + SW + (8 * g + q4) * 512 + (((8 * w + (p4 >> 1)) ^ ((q4 | ((g & 1) << 2)) << 1)) * 16) + (p4 & 1) * 8;
    unsigned xad[2] = {xa, xa ^ 64}, wad[2][4];
#pragma unroll
    for (int i = 0; i < 4; ++i) { wad[0][i] = wa ^ (i * 32); wad[1][i] = wad[0][i] + 65536; }
    asm volatile("" : "+v"(xad[0]), "+v"(xad[1]), "+v"(wad[0][0]), "+v"(wad[0][1]), "+v"(wad[0][2]), "+v"(wad[0][3]),
                      "+v"(wad[1][0]), "+v"(wad[1][1]), "+v"(wad[1][2]), "+v"(wad[1][3]));
    auto rd = [&](bf16x8 (&fa)[4], bf16x8 (&fb)[4], int slot, int half) {
#pragma unroll
      for (int i = 0; i < 4; ++i) {
        const unsigned wo_ = (slot & 1) * 32768 + half * 16384;
        fb[i] = ld128(xad[half] + slot * 8192 + i * 2048);
        fa[i] = ldtr(wad[slot >> 1][i] + wo_, wad[slot >> 1][i] + wo_ + 2048);
      }
    };
    auto mm16 = [&](const bf16x8 (&fa)[4], const bf16x8 (&fb)[4], bool first) {
#pragma unroll
      for (int i = 0; i < 4; ++i)
#pragma unroll
        for (int j = 0; j < 4; ++j) qc[i][j] = MFMA(fa[i], fb[j], first ? ZERO4 : qc[i][j]);
    };
#define SG_HALF16() do { SG_MFMA(1); SG_DSR(2); SG_MFMA(1); SG_DSR(2); _Pragma("unroll") for (int i_ = 0; i_ < 8; ++i_) { SG_MFMA(1); SG_DSR(1); } SG_MFMA(6); } while (0)
#define PIN_QC() asm volatile("" : "+v"(qc[0][0]), "+v"(qc[0][1]), "+v"(qc[0][2]), "+v"(qc[0][3]), "+v"(qc[1][0]), "+v"(qc[1][1]), "+v"(qc[1][2]), "+v"(qc[1][3]), \
                                   "+v"(qc[2][0]), "+v"(qc[2][1]), "+v"(qc[2][2]), "+v"(qc[2][3]), "+v"(qc[3][0]), "+v"(qc[3][1]), "+v"(qc[3][2]), "+v"(qc[3][3]))
    BAR_C();
    epv = __hip_atomic_load(epoch_p, __ATOMIC_RELAXED, __HIP_MEMORY_SCOPE_AGENT);
    rd(ga0, gb0, 0, 0);
    __builtin_amdgcn_sched_barrier(0);
#pragma unroll
    for (int t = 0; t < 7; ++t) {
      rd(ga1, gb1, t % 3, 1);
      mm16(ga0, gb0, t == 0);
      SG_HALF16();
      PIN_QC();
      BAR_C();
      if (t < 6) {
        rd(ga0, gb0, (t + 1) % 3, 0);
        mm16(ga1, gb1, false);
        SG_HALF16(); PIN_QC(); __builtin_amdgcn_sched_barrier(0);
      }
    }
#pragma unroll
    for (int i = 0; i <= 4; ++i) {
      if (i < 4) {
#pragma unroll
        for (int j = 0; j < 4; ++j) qc[i][j] = MFMA(ga1[i], gb1[j], qc[i][j]);
        asm volatile("" : "+v"(qc[i][0]), "+v"(qc[i][1]), "+v"(qc[i][2]), "+v"(qc[i][3]));
      }
      if (i > 0) {
#pragma unroll
        for (int j = 0; j < 4; ++j)
          *(LAS u32x2*)(size_t)(qa + j * 8192 + (i - 1) * 512) = u32x2{pk(qc[i - 1][j][0], qc[i - 1][j][1]), pk(qc[i - 1][j][2], qc[i - 1][j][3])};
        if (i < 4) { SG_MFMA(1); SG_VALU(2); SG_MFMA(1); SG_VALU(2); SG_DSW(1); SG_MFMA(1); SG_VALU(2); SG_DSW(1); SG_MFMA(1); SG_VALU(2); SG_DSW(2); }
      }
      __builtin_amdgcn_sched_barrier(0);
    }
  }
  BAR_C();
  const unsigned want = next_epoch(__builtin_amdgcn_readfirstlane(epv));
  u32x4 pv[2];
  {
    f32x4 sc[4];
    bf16x8 qf[8], kf[8][4];
    auto rdS = [&](int s) {
      qf[s] = ld128(own + s * 1024);
#pragma unroll
      for (int kt = 0; kt < 4; ++kt) kf[s][kt] = ld128(kad[s & 3] + kt * 8192 + (s >> 2) * 256);
      __builtin_amdgcn_sched_barrier(0);
    };
    rdS(0); rdS(1); rdS(2);
#pragma unroll
    for (int s = 0; s < 8; ++s) {
#pragma unroll
      for (int kt = 0; kt < 4; ++kt) sc[kt] = MFMA(kf[s][kt], qf[s], s == 0 ? ZERO4 : sc[kt]);
      asm volatile("" : "+v"(sc[0]), "+v"(sc[1]), "+v"(sc[2]), "+v"(sc[3]));
      __builtin_amdgcn_sched_barrier(0);
      if (s < 5) rdS(s + 3);
    }
    float mx = max3(max3(sc[0][0], sc[0][1], sc[0][2]), max3(sc[0][3], sc[1][0], sc[1][1]), max3(sc[1][2], sc[1][3], sc[2][0]));
    mx = max3(mx, max3(sc[2][1], sc[2][2], sc[2][3]), max3(max2(sc[3][0], sc[3][1]), sc[3][2], sc[3][3]));
    mx = xmax(mx);
    constexpr float C = 0.0625f * 1.4426950408889634f;
    const float mc = mx * C;
    float l = 0.f;
#pragma unroll
    for (int kt = 0; kt < 4; ++kt)
#pragma unroll
      for (int r = 0; r < 4; ++r) { sc[kt][r] = __builtin_amdgcn_exp2f(fmaf(sc[kt][r], C, -mc)); l += sc[kt][r]; }
    l = xsum(l);
    const float inv = __builtin_amdgcn_rcpf(l);
#pragma unroll
    for (int e = 0; e < 2; ++e) {
      pv[e] = __builtin_bit_cast(u32x4, pk8(sc[2 * e] * inv, sc[2 * e + 1] * inv));
      __builtin_amdgcn_raw_buffer_store_b128(pv[e], rs, pgo + e * 1024, 0, 16);
    }
  }
  if (m == 7) {
    asm volatile("s_waitcnt vmcnt(0)" ::: "memory");
    if (lane == 0) __hip_atomic_store(pflag_p + m * 4 + w, want, __ATOMIC_RELAXED, __HIP_MEMORY_SCOPE_AGENT);
    return;
  }
#define FENCE() __builtin_amdgcn_sched_barrier(0)
  f32x4 o[2][4];
  {
    f32x4 acc[4][4];
    bf16x8 fa0[4], fb0[4], fa1[4], fb1[4];
    auto rd = [&](bf16x8 (&fa)[4], bf16x8 (&fb)[4], int z, int half) {
      const int slot = zslot(z);
#pragma unroll
      for (int i = 0; i < 4; ++i) {
        const unsigned lo_ = (slot & 1) * 32768;
        fa[i] = ld128(vba[slot >> 1][half] + lo_ + i * 2048);
        const unsigned wa_ = wba[slot >> 1][i & 1] + lo_ + (i >> 1) * 8192 + half * 4096;
        fb[i] = ldtr(wa_, wa_ + 512);
      }
    };
    auto mm = [&](const bf16x8 (&fa)[4], const bf16x8 (&fb)[4], bool first) {
#pragma unroll
      for (int j = 0; j < 4; ++j)
#pragma unroll
        for (int i = 0; i < 4; ++i) acc[j][i] = MFMA(fa[i], fb[j], first ? ZERO4 : acc[j][i]);
    };
#define PIN_ACC() asm volatile("" : "+v"(acc[0][0]), "+v"(acc[0][1]), "+v"(acc[0][2]), "+v"(acc[0][3]), "+v"(acc[1][0]), "+v"(acc[1][1]), "+v"(acc[1][2]), "+v"(acc[1][3]), \
                                    "+v"(acc[2][0]), "+v"(acc[2][1]), "+v"(acc[2][2]), "+v"(acc[2][3]), "+v"(acc[3][0]), "+v"(acc[3][1]), "+v"(acc[3][2]), "+v"(acc[3][3]))
    bf16x8 cf0[4][2];
    BAR_C();
    rd(fa0, fb0, 0, 0);
    *(LAS u32x4*)(size_t)plo = pv[0];
    *(LAS u32x4*)(size_t)(plo + 1024) = pv[1];
    FENCE();
#pragma unroll
    for (int z = 0; z < 8; ++z) {
      rd(fa1, fb1, z, 1);
      mm(fa0, fb0, (z & 3) == 0);
      if (z == 4) asm volatile("" : "+v"(cf0[0][0]), "+v"(cf0[0][1]), "+v"(cf0[1][0]), "+v"(cf0[1][1]), "+v"(cf0[2][0]), "+v"(cf0[2][1]), "+v"(cf0[3][0]), "+v"(cf0[3][1]));
      SG_HALF16();
      PIN_ACC();
      if (z == 1) {
        asm volatile("s_waitcnt vmcnt(0)" ::: "memory");
        if (lane == 0) __hip_atomic_store(pflag_p + m * 4 + w, want, __ATOMIC_RELAXED, __HIP_MEMORY_SCOPE_AGENT);
      }
      BAR_C();
      if (z < 7) {
        rd(fa0, fb0, z + 1, 0);
        mm(fa1, fb1, false);
        if (z == 3) {
#pragma unroll
          for (int j = 0; j < 4; ++j) { cf0[j][0] = pk8(acc[j][0], acc[j][1]); cf0[j][1] = pk8(acc[j][2], acc[j][3]); }
          SG_MFMA(1); SG_DSR(2); SG_MFMA(1); SG_DSR(2);
#pragma unroll
          for (int i = 0; i < 8; ++i) { SG_MFMA(1); SG_DSR(1); }
#pragma unroll
          for (int i = 0; i < 6; ++i) { SG_MFMA(1); SG_VALU(2); }
          SG_VALU(20);
        } else {
          SG_HALF16();
          PIN_ACC();
        }
        FENCE();
      }
    }
    {
      bf16x8 pf[2][4][2][2], cf[4][2];
      auto rdP = [&](int G, int qt) {
#pragma unroll
        for (int i = 0; i < 4; ++i) pf[G][qt][i >> 1][i & 1] = ld128(pad[qt] + G * 16384 + (i >> 1) * 8192 + (i & 1) * 1024);
      };
      auto pc = [&](int G, int qt) {
#pragma unroll
        for (int i = 0; i < 4; ++i)
#pragma unroll
          for (int nt = 0; nt < 2; ++nt)
            o[nt][qt] = MFMA((G ? cf : cf0)[(i & 2) + nt][i & 1], pf[G][qt][i >> 1][i & 1], (G == 0 && i == 0) ? ZERO4 : o[nt][qt]);
      };
      auto pin = [&](int qt) { asm volatile("" : "+v"(o[0][qt]), "+v"(o[1][qt])); FENCE(); };
      auto send = [&](int qt) {
        __builtin_amdgcn_raw_buffer_store_b128(__builtin_bit_cast(u32x4, pk8(o[0][qt], o[1][qt])), rs, fo + ((gp ^ qt) << 14), 0, 16);
        FENCE();
      };
#define SG_PC(nr, nv) do { _Pragma("unroll") for (int i_ = 0; i_ < 4; ++i_) { SG_MFMA(1); if (nr) SG_DSR(1); SG_MFMA(1); if (nv) SG_VALU(nv); } } while (0)
#pragma unroll
      for (int j = 0; j < 4; ++j) {
#pragma unroll
        for (int i = 0; i < 4; ++i) acc[j][i] = MFMA(fa1[i], fb1[j], acc[j][i]);
        if (j == 0) rdP(0, 3);
        if (j == 1) rdP(0, 2);
        if (j == 2) rdP(1, 3);
        if (j == 3) rdP(1, 2);
#pragma unroll
        for (int i = 0; i < 4; ++i) { SG_MFMA(1); SG_DSR(1); }
        asm volatile("" : "+v"(acc[j][0]), "+v"(acc[j][1]), "+v"(acc[j][2]), "+v"(acc[j][3]));
        FENCE();
      }
      pc(0, 3); rdP(0, 1);
      cf[0][0] = pk8(acc[0][0], acc[0][1]); cf[0][1] = pk8(acc[0][2], acc[0][3]); cf[1][0] = pk8(acc[1][0], acc[1][1]); cf[1][1] = pk8(acc[1][2], acc[1][3]);
      SG_PC(1, 4);
      asm volatile("" : "+v"(cf[0][0]), "+v"(cf[0][1]), "+v"(cf[1][0]), "+v"(cf[1][1])); pin(3);
      pc(0, 2); rdP(1, 1);
      cf[2][0] = pk8(acc[2][0], acc[2][1]); cf[2][1] = pk8(acc[2][2], acc[2][3]); cf[3][0] = pk8(acc[3][0], acc[3][1]); cf[3][1] = pk8(acc[3][2], acc[3][3]);
      SG_PC(1, 4);
      asm volatile("" : "+v"(cf[2][0]), "+v"(cf[2][1]), "+v"(cf[3][0]), "+v"(cf[3][1])); pin(2);
      pc(1, 3); rdP(0, 0); SG_PC(1, 0); pin(3);
      pc(1, 2); rdP(1, 0); SG_PC(1, 0); pin(2);
      send(3);
      pc(0, 1); pin(1);
      send(2);
      pc(1, 1); pin(1);
      pc(0, 0); pin(0);
      send(1);
      pc(1, 0); pin(0);
    }
  }
  asm volatile("s_waitcnt vmcnt(0)" ::: "memory");
  if (lane < 4 && lane != gp) __hip_atomic_store(fs, want, __ATOMIC_RELAXED, __HIP_MEMORY_SCOPE_AGENT);
  {
    u32x4 d[3];
    unsigned f0, f1, f2, cnt;
#define POLL(f) "global_atomic_add %[" #f "], %[fp], %[zero], off sc0 sc1\n\t"
#define TEST(f) "s_waitcnt vmcnt(2)\n\tv_cmp_eq_u32 vcc, %[want], %[" #f "]\n\ts_nop 1\n\ts_or_b64 vcc, vcc, %[self]\n\ts_cmp_eq_u64 vcc, exec\n\ts_cbranch_scc1 2f\n\t"
    asm volatile("s_mov_b64 exec, 15\n\t"
                 POLL(f0) "s_sleep 6\n\t" POLL(f1) "s_sleep 6\n\t" POLL(f2) "s_mov_b32 %[cnt], %[cap]\n"
                 "1:\n\t" TEST(f0) POLL(f0) TEST(f1) POLL(f1) TEST(f2) POLL(f2)
                 "s_sub_u32 %[cnt], %[cnt], 1\n\ts_cbranch_scc0 1b\n"
                 "2:\n\t"
                 "s_mov_b64 exec, -1\n\t"
                 "buffer_load_dwordx4 %[d0], %[fi], %[rs], %[s0] offen sc1\n\t"
                 "buffer_load_dwordx4 %[d1], %[fi], %[rs], %[s1] offen sc1\n\t"
                 "buffer_load_dwordx4 %[d2], %[fi], %[rs], %[s2] offen sc1\n\t"
                 "s_waitcnt vmcnt(0)"
                 : [f0] "=&v"(f0), [f1] "=&v"(f1), [f2] "=&v"(f2), [d0] "=&v"(d[0]), [d1] "=&v"(d[1]), [d2] "=&v"(d[2]), [cnt] "=&s"(cnt)
                 : [fp] "v"(fp), [want] "s"(want), [self] "s"(1ull << gp), [cap] "i"(SPIN_CAP), [fi] "v"(fi), [rs] "s"(rs), [zero] "v"(0),
                   [s0] "s"((gp ^ 1) * 4096), [s1] "s"((gp ^ 2) * 4096), [s2] "s"((gp ^ 3) * 4096)
                 : "vcc", "scc", "memory");
    float a[8];
#pragma unroll
    for (int i = 0; i < 4; ++i) { a[i] = o[0][0][i]; a[4 + i] = o[1][0][i]; }
#pragma unroll
    for (int k = 0; k < 3; ++k)
#pragma unroll
      for (int i = 0; i < 4; ++i) { a[2 * i] += __uint_as_float(d[k][i] << 16); a[2 * i + 1] += __uint_as_float(d[k][i] & 0xffff0000u); }
    const auto s0 = __builtin_amdgcn_permlane16_swap(pk(a[0], a[1]), pk(a[4], a[5]), false, false);
    const auto s1 = __builtin_amdgcn_permlane16_swap(pk(a[2], a[3]), pk(a[6], a[7]), false, false);
    *op = u32x4{s0[0], s1[0], s0[1], s1[1]};
    if (gp == 2 && m == 0 && w == 0 && lane == 0) __hip_atomic_store(epoch_p, want, __ATOMIC_RELAXED, __HIP_MEMORY_SCOPE_AGENT);
  }
}

extern "C" void kernel_launch(void* const* d_in, const int* in_sizes, int n_in, void* d_out, int out_size,
                              void* d_ws, size_t ws_size, hipStream_t stream) {
  Params p;
  p.x0 = (const char*)d_in[0]; p.k0 = (const char*)d_in[1]; p.v0 = (const char*)d_in[2]; p.wq0 = (const char*)d_in[3]; p.wo0 = (const char*)d_in[4];
  p.x1 = (const char*)d_in[5]; p.k1 = (const char*)d_in[6]; p.v1 = (const char*)d_in[7]; p.wq1 = (const char*)d_in[8]; p.wo1 = (const char*)d_in[9];
  p.out0 = p.out1 = (char*)d_out;
  p.ws0 = p.ws0b = p.ws1 = p.ws1b = (char*)d_ws;
  gqa_fused<<<256, 512, 0, stream>>>(p);
}
```
